```python
import torch

def _softmax(x, axis=-1):
    return torch.softmax(x, dim=axis)
def _gelu(x):
    return torch.nn.functional.gelu(x, approximate="tanh")
def _silu(x):
    return torch.nn.functional.silu(x)
def _sigmoid(x):
    return torch.sigmoid(x)
def _layernorm(x, axis=-1, eps=1e-5):
    m = x.mean(dim=axis, keepdim=True)
    v = x.var(dim=axis, keepdim=True, unbiased=False)
    return (x - m) / torch.sqrt(v + eps)
def _scatter_add(tgt, idx, src, axis=0):
    return tgt.clone().index_add_(axis, idx, src)
def _segment_sum(src, seg, n):
    out = torch.zeros((n,) + src.shape[1:], dtype=src.dtype)
    return out.index_add_(0, seg, src)
def _pad(x, pw):
    flat = tuple(v for pair in reversed(pw) for v in pair)
    return torch.nn.functional.pad(x, flat)

kernel_name = 'standalone__linear_kernel_hedgehog__prompt'

def setup_inputs():

    _w = (lambda a: (lambda t: t.to(torch.bfloat16) if t.is_floating_point() else t)(torch.as_tensor(a)))
    B, S, D, H = 1, 2048, 1024, 32
    Dh = D // H
    return {
        "x": _w((lambda *s: torch.randn(*s, dtype=torch.bfloat16) * 0.4)(B, S, D)),
        "wqkv": _w((lambda *s: torch.randn(*s, dtype=torch.bfloat16) * 0.4)(D, 3 * D)),
        "w_phi": _w((lambda *s: torch.randn(*s, dtype=torch.bfloat16) * 0.4)(H, Dh, Dh) * (Dh ** -0.5)),
        "wo": _w((lambda *s: torch.randn(*s, dtype=torch.bfloat16) * 0.05)(D, D)),
    }

def reference(x, wqkv, w_phi, wo):
    B, S, D = x.shape
    H = 32; Dh = D // H
    qkv = (lambda x, *d: x.permute(*d))((x @ wqkv).reshape(B, S, 3, H, Dh), 2, 0, 3, 1, 4)
    q, k, v = qkv[0], qkv[1], qkv[2]
    pq = torch.einsum("bhsd,hdf->bhsf", q, w_phi)
    pk = torch.einsum("bhsd,hdf->bhsf", k, w_phi)
    phi_q = torch.concatenate([_softmax(pq, axis=-1), _softmax(-pq, axis=-1)], axis=-1)
    phi_k = torch.concatenate([_softmax(pk, axis=-1), _softmax(-pk, axis=-1)], axis=-1)
    kv = torch.einsum("bhsf,bhsd->bhfd", phi_k, v)
    z  = torch.einsum("bhsf->bhf", phi_k)
    num = torch.einsum("bhqf,bhfd->bhqd", phi_q, kv)
    den = torch.einsum("bhqf,bhf->bhq", phi_q, z)[..., None] + 1e-6
    o = num / den
    return (lambda x, *d: x.permute(*d))(o, 0, 2, 1, 3).reshape(B, S, D) @ wo
```

```cpp
#include <hip/hip_runtime.h>
#include <hip/hip_bf16.h>
#include <stdint.h>
namespace naive {
typedef unsigned short u16;
__device__ __forceinline__ float b2f(u16 x){ return __uint_as_float(((unsigned)x)<<16); }
__device__ __forceinline__ u16 f2b(float f){ unsigned u=__float_as_uint(f); u += 0x7fffu + ((u>>16)&1u); return (u16)(u>>16); }
__device__ __forceinline__ float rb(float f){ return b2f(f2b(f)); }
constexpr int S=2048, D=1024, H=32, DH=32;
__global__ void gemm(const u16* A, const u16* B, u16* C, int M, int N, int K){
  __shared__ float As[32][33], Bs[32][33];
  int tx=threadIdx.x&31, ty=threadIdx.x>>5;
  int row0=blockIdx.y*32, col0=blockIdx.x*32;
  float acc[4]={0,0,0,0};
  for(int k0=0;k0<K;k0+=32){
    for(int i=0;i<4;i++){ As[ty+8*i][tx]=b2f(A[(size_t)(row0+ty+8*i)*K+k0+tx]); Bs[ty+8*i][tx]=b2f(B[(size_t)(k0+ty+8*i)*N+col0+tx]); }
    __syncthreads();
    for(int k=0;k<32;k++){ float b=Bs[k][tx]; for(int i=0;i<4;i++) acc[i]+=As[ty+8*i][k]*b; }
    __syncthreads();
  }
  for(int i=0;i<4;i++) C[(size_t)(row0+ty+8*i)*N+col0+tx]=f2b(acc[i]);
}
__global__ __launch_bounds__(64) void phi_k(const u16* qkv, const u16* wphi, u16* phi){
  int idx=blockIdx.x*blockDim.x+threadIdx.x;
  int s=idx%S, h=(idx/S)%H, w=idx/(S*H);
  float q[32], p[32];
  #pragma unroll
  for(int d=0;d<32;d++) q[d]=b2f(qkv[(size_t)s*3*D+w*D+h*32+d]);
  float mx=-1e30f, mn=1e30f;
  #pragma unroll
  for(int f=0;f<32;f++) p[f]=0;
  #pragma unroll
  for(int d=0;d<32;d++){
    #pragma unroll
    for(int f=0;f<32;f++) p[f]+=q[d]*b2f(wphi[(h*32+d)*32+f]);
    asm volatile("" ::: "memory");
  }
  #pragma unroll
  for(int f=0;f<32;f++){ p[f]=rb(p[f]); mx=fmaxf(mx,p[f]); mn=fminf(mn,p[f]); }
  float s1=0,s2=0;
  #pragma unroll
  for(int f=0;f<32;f++){ s1+=expf(p[f]-mx); s2+=expf(mn-p[f]); }
  u16* o=phi+(size_t)idx*64;
  #pragma unroll
  for(int f=0;f<32;f++){ o[f]=f2b(expf(p[f]-mx)/s1); o[32+f]=f2b(expf(mn-p[f])/s2); }
}
__global__ void kv_k(const u16* qkv, const u16* phi, float* kv, float* z){
  int h=blockIdx.x/64, f=blockIdx.x%64; int d=threadIdx.x;
  const u16* pk=phi+(size_t)(S*H+h*S)*64;
  float a=0;
  if(d<32){ for(int s=0;s<S;s++) a+=b2f(pk[s*64+f])*b2f(qkv[(size_t)s*3*D+2*D+h*32+d]); kv[(h*64+f)*32+d]=rb(a); }
  else if(d==32){ for(int s=0;s<S;s++) a+=b2f(pk[s*64+f]); z[h*64+f]=rb(a); }
}
__global__ void o_k(const u16* phi, const float* kv, const float* z, u16* o){
  int idx=blockIdx.x*blockDim.x+threadIdx.x;
  int d=idx%32, h=(idx/32)%32, s=idx/1024;
  const u16* pq=phi+(size_t)(h*S+s)*64;
  float n=0, de=0;
  for(int f=0;f<64;f++){ float p=b2f(pq[f]); n+=p*kv[(h*64+f)*32+d]; de+=p*z[h*64+f]; }
  n=rb(n); de=rb(rb(de)+1e-6f);
  o[idx]=f2b(n/de);
}
inline void run(const u16* x, const u16* wqkv, const u16* wphi, const u16* wo, u16* out, char* ws, hipStream_t st){
  u16* qkv=(u16*)ws; ws+=(size_t)S*3*D*2;
  u16* phi=(u16*)ws; ws+=(size_t)2*H*S*64*2;
  float* kv=(float*)ws; ws+=H*64*32*4;
  float* z=(float*)ws; ws+=H*64*4;
  u16* o=(u16*)ws;
  gemm<<<dim3(3*D/32,S/32),256,0,st>>>(x,wqkv,qkv,S,3*D,D);
  phi_k<<<2*H*S/64,64,0,st>>>(qkv,wphi,phi);
  kv_k<<<H*64,64,0,st>>>(qkv,phi,kv,z);
  o_k<<<S*D/256,256,0,st>>>(phi,kv,z,o);
  gemm<<<dim3(D/32,S/32),256,0,st>>>(o,wo,out,S,D,D);
}
}
extern "C" void kernel_launch(void* const* d_in, const int* in_sizes, int n_in, void* d_out, int out_size, void* d_ws, size_t ws_size, hipStream_t stream) {
  naive::run((const naive::u16*)d_in[0],(const naive::u16*)d_in[1],(const naive::u16*)d_in[2],(const naive::u16*)d_in[3],(naive::u16*)d_out,(char*)d_ws,stream);
}
```

```cpp
#include <hip/hip_runtime.h>
#include <hip/hip_bf16.h>
#include <stdint.h>
namespace naive {
typedef unsigned short u16;
__device__ __forceinline__ float b2f(u16 x){ return __uint_as_float(((unsigned)x)<<16); }
__device__ __forceinline__ u16 f2b(float f){ unsigned u=__float_as_uint(f); u += 0x7fffu + ((u>>16)&1u); return (u16)(u>>16); }
__device__ __forceinline__ float rb(float f){ return b2f(f2b(f)); }
constexpr int S=2048, D=1024, H=32, DH=32;
__global__ void gemm(const u16* A, const u16* B, u16* C, int M, int N, int K){
  __shared__ float As[32][33], Bs[32][33];
  int tx=threadIdx.x&31, ty=threadIdx.x>>5;
  int row0=blockIdx.y*32, col0=blockIdx.x*32;
  float acc[4]={0,0,0,0};
  for(int k0=0;k0<K;k0+=32){
    for(int i=0;i<4;i++){ As[ty+8*i][tx]=b2f(A[(size_t)(row0+ty+8*i)*K+k0+tx]); Bs[ty+8*i][tx]=b2f(B[(size_t)(k0+ty+8*i)*N+col0+tx]); }
    __syncthreads();
    for(int k=0;k<32;k++){ float b=Bs[k][tx]; for(int i=0;i<4;i++) acc[i]+=As[ty+8*i][k]*b; }
    __syncthreads();
  }
  for(int i=0;i<4;i++) C[(size_t)(row0+ty+8*i)*N+col0+tx]=f2b(acc[i]);
}
__global__ __launch_bounds__(64) void phi_k(const u16* qkv, const u16* wphi, u16* phi){
  int idx=blockIdx.x*blockDim.x+threadIdx.x;
  int s=idx%S, h=(idx/S)%H, w=idx/(S*H);
  float q[32], p[32];
  #pragma unroll
  for(int d=0;d<32;d++) q[d]=b2f(qkv[(size_t)s*3*D+w*D+h*32+d]);
  float mx=-1e30f, mn=1e30f;
  #pragma unroll
  for(int f=0;f<32;f++) p[f]=0;
  #pragma unroll
  for(int d=0;d<32;d++){
    #pragma unroll
    for(int f=0;f<32;f++) p[f]+=q[d]*b2f(wphi[(h*32+d)*32+f]);
    asm volatile("" ::: "memory");
  }
  #pragma unroll
  for(int f=0;f<32;f++){ p[f]=rb(p[f]); mx=fmaxf(mx,p[f]); mn=fminf(mn,p[f]); }
  float s1=0,s2=0;
  #pragma unroll
  for(int f=0;f<32;f++){ s1+=expf(p[f]-mx); s2+=expf(mn-p[f]); }
  u16* o=phi+(size_t)idx*64;
  #pragma unroll
  for(int f=0;f<32;f++){ o[f]=f2b(expf(p[f]-mx)/s1); o[32+f]=f2b(expf(mn-p[f])/s2); }
}
__global__ void kv_k(const u16* qkv, const u16* phi, float* kv, float* z){
  int h=blockIdx.x/64, f=blockIdx.x%64; int d=threadIdx.x;
  const u16* pk=phi+(size_t)(S*H+h*S)*64;
  float a=0;
  if(d<32){ for(int s=0;s<S;s++) a+=b2f(pk[s*64+f])*b2f(qkv[(size_t)s*3*D+2*D+h*32+d]); kv[(h*64+f)*32+d]=rb(a); }
  else if(d==32){ for(int s=0;s<S;s++) a+=b2f(pk[s*64+f]); z[h*64+f]=rb(a); }
}
__global__ void o_k(const u16* phi, const float* kv, const float* z, u16* o){
  int idx=blockIdx.x*blockDim.x+threadIdx.x;
  int d=idx%32, h=(idx/32)%32, s=idx/1024;
  const u16* pq=phi+(size_t)(h*S+s)*64;
  float n=0, de=0;
  for(int f=0;f<64;f++){ float p=b2f(pq[f]); n+=p*kv[(h*64+f)*32+d]; de+=p*z[h*64+f]; }
  n=rb(n); de=rb(rb(de)+1e-6f);
  o[idx]=f2b(n/de);
}
inline void run(const u16* x, const u16* wqkv, const u16* wphi, const u16* wo, u16* out, char* ws, hipStream_t st){
  u16* qkv=(u16*)ws; ws+=(size_t)S*3*D*2;
  u16* phi=(u16*)ws; ws+=(size_t)2*H*S*64*2;
  float* kv=(float*)ws; ws+=H*64*32*4;
  float* z=(float*)ws; ws+=H*64*4;
  u16* o=(u16*)ws;
  gemm<<<dim3(3*D/32,S/32),256,0,st>>>(x,wqkv,qkv,S,3*D,D);
  phi_k<<<2*H*S/64,64,0,st>>>(qkv,wphi,phi);
  kv_k<<<H*64,64,0,st>>>(qkv,phi,kv,z);
  o_k<<<S*D/256,256,0,st>>>(phi,kv,z,o);
  gemm<<<dim3(D/32,S/32),256,0,st>>>(o,wo,out,S,D,D);
}
}
typedef unsigned short u16;
typedef __attribute__((address_space(3))) void* ldsp;
#define WAITV(n) asm volatile("s_waitcnt vmcnt(" #n ")" ::: "memory")
template<int N> __device__ __forceinline__ void waitv(){ asm volatile("s_waitcnt vmcnt(%0)"::"n"(N):"memory"); }

template<int MODE,int NW,int NS,int MAP,int SEL,int PF>
__global__ __launch_bounds__(NW*64) void loadk(const u16* x, const u16* w, unsigned long long* stamps, unsigned* sink){
  extern __shared__ __attribute__((aligned(16))) char smem[];
  unsigned long long t0 = __builtin_amdgcn_s_memrealtime();
  unsigned long long c0 = __builtin_amdgcn_s_memtime();
  const int b = blockIdx.x; const int xcd = b&7, i = b>>3;
  int rt, ct;
  constexpr int NA = MODE==0?16:32, NB = MODE==0?24:12, NP=NA+NB;
  constexpr int STAGE = NP*1024;
  if (MODE==0){
    if (MAP==0){ rt = (xcd&1)*8 + (i&7); ct = (xcd>>1)*4 + (i>>3);}
    else if (MAP==1){ rt = (xcd&3)*4 + (i&3); ct = (xcd>>2)*8 + (i>>2);}
    else if (MAP==2){ rt = xcd*2 + (i&1); ct = i>>1; }
    else { rt = b>>4; ct = b&15; }
  } else {
    if (MAP==0){ rt = (xcd&1)*4 + (i&3); ct = (xcd>>1)*8 + (i>>2);}
    else if (MAP==1){ rt = (xcd&3)*2 + (i&1); ct = (xcd>>2)*16 + (i>>1);}
    else if (MAP==2){ rt = xcd; ct = i; }
    else { rt = i&7; ct = xcd*4 + (i>>3); }
  }
  const int wave = __builtin_amdgcn_readfirstlane(threadIdx.x>>6), lane = threadIdx.x&63;
  auto rx = __builtin_amdgcn_make_buffer_rsrc((void*)x, 0, (SEL&1)?2048*1024*2:0, 0x00020000);
  auto rw = __builtin_amdgcn_make_buffer_rsrc((void*)w, 0, (SEL&2)?1024*3072*2:0, 0x00020000);
  constexpr int PPW = NP/NW;
  unsigned va0 = (lane>>3)*2048 + (((lane&7) ^ ((lane>>4)&7))<<4);
  unsigned va1 = (lane>>3)*2048 + (((lane&7) ^ ((4+(lane>>4))&7))<<4);
  unsigned vb = MODE==0 ? (lane>>3)*6144 + (((lane&7) ^ (((lane>>4)&1)<<2))<<4) : (lane>>2)*6144 + ((lane&3)<<4);
  auto issue = [&](int kt){
    int buf = kt % NS;
    #pragma unroll
    for (int p=0;p<PPW;p++){
      int pi = wave + p*NW;
      char* dst = smem + buf*STAGE + pi*1024;
      if (p < NA/NW){
        unsigned so = (rt*(MODE==0?128:256) + pi*8)*2048 + kt*128;
        __builtin_amdgcn_raw_ptr_buffer_load_lds(rx, (ldsp)dst, 16, (pi&1)?va1:va0, so, 0, 0);
      } else {
        int j = pi-NA;
        unsigned so;
        if (MODE==0){ int g=j>>3, jj=j&7; so = (kt*64+jj*8)*6144 + g*2048 + ct*128; }
        else { int g=j>>2, jj=j&3; so = (kt*64+jj*16)*6144 + g*2048 + ct*64; }
        __builtin_amdgcn_raw_ptr_buffer_load_lds(rw, (ldsp)dst, 16, vb, so, 0, 0);
      }
    }
  };
  typedef unsigned u32x4 __attribute__((ext_vector_type(4)));
  u32x4 pv[8];
  #pragma unroll
  for(int j=0;j<8;j++) pv[j]=(u32x4){0,0,0,0};
  auto rxf = __builtin_amdgcn_make_buffer_rsrc((void*)x, 0, 2048*1024*2, 0x00020000);
  auto rwf = __builtin_amdgcn_make_buffer_rsrc((void*)w, 0, 1024*3072*2, 0x00020000);
  #define PFD(dst,rs,off) asm volatile("buffer_load_dword %0, %1, %2, 0 offen" : "+v"(dst) : "v"(off), "s"(rs) : "memory")
  #define PFQ(dst,rs,off) asm volatile("buffer_load_dwordx4 %0, %1, %2, 0 offen" : "+v"(dst) : "v"(off), "s"(rs) : "memory")
  asm volatile("s_nop 4");
  if (PF==1 || PF==2){ if (wave<2) PFD(pv[0].x, rxf, (unsigned)(b*16384 + (wave*64+lane)*128)); }
  if (PF==2){ if (wave<3) PFD(pv[1].x, rwf, (unsigned)(b*24576 + (wave*64+lane)*128)); }
  if (PF==3){
    #pragma unroll
    for(int j=0;j<8/NW;j++) PFD(pv[j].x, rxf, (unsigned)(((xcd&1)*1024 + i*32)*2048 + ((wave*(8/NW)+j)*64+lane)*128));
  }
  if (PF==4){
    #pragma unroll
    for(int j=0;j<16/NW;j++) PFQ(pv[j], rxf, (unsigned)(b*16384 + ((wave*(16/NW)+j)*64+lane)*16));
  }
  for (int t=0;t<NS-1;t++) issue(t);
  unsigned long long tfirst=0;
  for (int t=0;t<16;t++){
    if (t+NS-1<16){ issue(t+NS-1); waitv<PPW*(NS-1)>(); }
    else if (t+NS-1==16) waitv<PPW*(NS-2)>();
    else if (NS>=3 && t+NS-1==17) waitv<PPW*(NS>=3?NS-3:0)>();
    else waitv<0>();
    __builtin_amdgcn_s_barrier();
    if (t==0) tfirst = __builtin_amdgcn_s_memrealtime();
  }
  waitv<0>();
  #pragma unroll
  for(int j=0;j<8;j++) asm volatile("" :: "v"(pv[j]));
  unsigned long long t1 = __builtin_amdgcn_s_memrealtime();
  unsigned long long c1 = __builtin_amdgcn_s_memtime();
  if (threadIdx.x==0){ stamps[b*4]=t0; stamps[b*4+1]=t1; stamps[b*4+2]=c1-c0; stamps[b*4+3]=tfirst; sink[b]=*(unsigned*)(smem+(b&63)*4); }
}

__global__ void spin(const unsigned long long* st){
  unsigned long long mn=~0ull,mx=0;
  for(int b=0;b<256;b++){ unsigned long long a=st[b*4], e=st[b*4+1]; mn=a<mn?a:mn; mx=e>mx?e:mx; }
  unsigned long long d=(mx-mn)*0; if(d>500000) d=500000;
  unsigned long long t0=__builtin_amdgcn_s_memrealtime();
  while(__builtin_amdgcn_s_memrealtime()-t0<d) __builtin_amdgcn_s_sleep(1);
}
extern "C" void kernel_launch(void* const* d_in, const int* in_sizes, int n_in, void* d_out, int out_size, void* d_ws, size_t ws_size, hipStream_t stream) {
  char* ws=(char*)d_ws;
  unsigned long long* st=(unsigned long long*)(ws+(100<<20));
  unsigned* sink=(unsigned*)(ws+(101<<20));
  hipFuncSetAttribute((const void*)loadk<0,4,4,0,3,0>, hipFuncAttributeMaxDynamicSharedMemorySize, 163840);
  loadk<0,4,4,0,3,0><<<256,256,163840,stream>>>((const u16*)d_in[0],(const u16*)d_in[1],st,sink);
  spin<<<1,64,0,stream>>>(st);
  naive::run((const naive::u16*)d_in[0],(const naive::u16*)d_in[1],(const naive::u16*)d_in[2],(const naive::u16*)d_in[3],(naive::u16*)d_out,(char*)d_ws,stream);
}
```

```cpp
#include <hip/hip_runtime.h>
#include <hip/hip_bf16.h>
#include <stdint.h>

typedef unsigned short u16;
typedef unsigned long long u64;
typedef __bf16 bf16x2 __attribute__((ext_vector_type(2)));
typedef __bf16 bf16x4 __attribute__((ext_vector_type(4)));
typedef __bf16 bf16x8 __attribute__((ext_vector_type(8)));
typedef float f32x2 __attribute__((ext_vector_type(2)));
typedef float f32x4 __attribute__((ext_vector_type(4)));
typedef float f32x16 __attribute__((ext_vector_type(16)));
typedef unsigned u32x2 __attribute__((ext_vector_type(2)));
typedef unsigned u32x4 __attribute__((ext_vector_type(4)));
#define LDS3 __attribute__((address_space(3)))
#define DI __device__ __forceinline__
#define MFMA(a, b, c) __builtin_amdgcn_mfma_f32_32x32x16_bf16((a), (b), (c), 0, 0, 0)
#define SCHED0() __builtin_amdgcn_sched_barrier(0)
#define STAMP(i)

template <int N> DI void waitv() { asm volatile("s_waitcnt vmcnt(%0)" ::"n"(N) : "memory"); }
DI void bar() { asm volatile("s_barrier" ::: "memory"); }
DI void lgkm0_bar() { asm volatile("s_waitcnt lgkmcnt(0)\n\ts_barrier" ::: "memory"); }
DI bf16x8 ld128(unsigned a) { return *(LDS3 const bf16x8*)(size_t)a; }
DI void st128(unsigned a, bf16x8 v) { *(LDS3 bf16x8*)(size_t)a = v; }
DI bf16x4 trd(unsigned a) { return __builtin_amdgcn_ds_read_tr16_b64_v4bf16((LDS3 bf16x4*)(size_t)a); }
DI bf16x8 tr2(unsigned a0, unsigned a1) { return __builtin_shufflevector(trd(a0), trd(a1), 0, 1, 2, 3, 4, 5, 6, 7); }
DI unsigned pk2(float lo, float hi) { f32x2 t = {lo, hi}; return __builtin_bit_cast(unsigned, __builtin_convertvector(t, bf16x2)); }
template <int S> DI bf16x8 pack8(const f32x16& x) {
  u32x4 p = {pk2(x[8 * S], x[8 * S + 1]), pk2(x[8 * S + 2], x[8 * S + 3]), pk2(x[8 * S + 4], x[8 * S + 5]), pk2(x[8 * S + 6], x[8 * S + 7])};
  return __builtin_bit_cast(bf16x8, p);
}
DI float xhalf_max(float v) { auto r = __builtin_amdgcn_permlane32_swap(__float_as_uint(v), __float_as_uint(v), false, false); return __builtin_fmaxf(__uint_as_float(r[0]), __uint_as_float(r[1])); }
DI float xhalf_min(float v) { auto r = __builtin_amdgcn_permlane32_swap(__float_as_uint(v), __float_as_uint(v), false, false); return __builtin_fminf(__uint_as_float(r[0]), __uint_as_float(r[1])); }
DI float xhalf_add(float v) { auto r = __builtin_amdgcn_permlane32_swap(__float_as_uint(v), __float_as_uint(v), false, false); return __uint_as_float(r[0]) + __uint_as_float(r[1]); }

constexpr int NS = 3, STAGE = 40960, BOFF = 16384, WPHI_OFF = NS * STAGE;
constexpr int K1_LDS = WPHI_OFF + 4096;
constexpr int HQ_OFF = 0, VF_OFF = 16384, PK_OFF = 32768;

DI void feature_map(const bf16x8 (&wf)[2], const bf16x8 (&in)[2], bf16x8 (&P)[4]) {
  f32x16 p = {};
  p = MFMA(wf[0], in[0], p);
  p = MFMA(wf[1], in[1], p);
  float mx = p[0], mn = p[0];
#pragma unroll
  for (int i = 1; i < 16; i++) { mx = __builtin_fmaxf(mx, p[i]); mn = __builtin_fminf(mn, p[i]); }
  mx = xhalf_max(mx); mn = xhalf_min(mn);
  const float c = 1.4426950408889634f;
  const float a = -mx * c, b = mn * c;
  f32x16 e, g; float s0 = 0.f, s1 = 0.f, t0 = 0.f, t1 = 0.f;
#pragma unroll
  for (int i = 0; i < 16; i++) { e[i] = __builtin_amdgcn_exp2f(__builtin_fmaf(p[i], c, a)); g[i] = __builtin_amdgcn_exp2f(__builtin_fmaf(p[i], -c, b)); }
#pragma unroll
  for (int i = 0; i < 16; i += 2) { s0 += e[i]; s1 += e[i + 1]; t0 += g[i]; t1 += g[i + 1]; }
  const float rs = __builtin_amdgcn_rcpf(xhalf_add(s0 + s1)), rt = __builtin_amdgcn_rcpf(xhalf_add(t0 + t1));
#pragma unroll
  for (int i = 0; i < 16; i++) { e[i] *= rs; g[i] *= rt; }
  P[0] = pack8<0>(e); P[1] = pack8<1>(e); P[2] = pack8<0>(g); P[3] = pack8<1>(g);
}

struct Frag { bf16x8 x0, x1, wq, wk, wv; };
template <int S> DI void load_frag(Frag& f, unsigned a, unsigned b) {
  f.x0 = ld128(a);
  f.wq = tr2(b + S * 2048, b + S * 2048 + 512);
  f.wk = tr2(b + 8192 + S * 2048, b + 8192 + S * 2048 + 512);
  f.wv = tr2(b + 16384 + S * 2048, b + 16384 + S * 2048 + 512);
  f.x1 = ld128(a + 4096);
}
struct Acc { f32x16 q0, q1, k0, k1, v0, v1; };
DI void mma6(Acc& c, const Frag& f) {
  c.q0 = MFMA(f.wq, f.x0, c.q0);
  c.k0 = MFMA(f.wk, f.x0, c.k0);
  c.v0 = MFMA(f.x0, f.wv, c.v0);
  c.q1 = MFMA(f.wq, f.x1, c.q1);
  c.k1 = MFMA(f.wk, f.x1, c.k1);
  c.v1 = MFMA(f.x1, f.wv, c.v1);
}
DI void interleave() {
  __builtin_amdgcn_sched_group_barrier(0x8, 1, 0); __builtin_amdgcn_sched_group_barrier(0x100, 2, 0);
  __builtin_amdgcn_sched_group_barrier(0x8, 1, 0); __builtin_amdgcn_sched_group_barrier(0x100, 2, 0);
  __builtin_amdgcn_sched_group_barrier(0x8, 1, 0); __builtin_amdgcn_sched_group_barrier(0x100, 2, 0);
  __builtin_amdgcn_sched_group_barrier(0x8, 1, 0); __builtin_amdgcn_sched_group_barrier(0x100, 2, 0);
  __builtin_amdgcn_sched_group_barrier(0x8, 2, 0);
}

DI void tile_of_block(int b, int& rt, int& ct) {
  const int xcd = b & 7, i = b >> 3;
  rt = (xcd & 1) * 8 + (i & 7); ct = (xcd >> 1) * 4 + (i >> 3);
}

__global__ __launch_bounds__(512, 2) void k1(const u16* __restrict__ x, const u16* __restrict__ w, const u16* __restrict__ wphi,
                                             u32x4* __restrict__ phiq, float* __restrict__ kvp, float* __restrict__ zp, u64* stamps) {
  extern __shared__ __attribute__((aligned(16))) char smem[];
  const unsigned lds = (unsigned)(size_t)(LDS3 char*)smem;
  const int b = blockIdx.x; int rt, ct; tile_of_block(b, rt, ct);
  const int wave = __builtin_amdgcn_readfirstlane(threadIdx.x >> 6), lane = threadIdx.x & 63;
  const int cw = wave & 3, wr = cw >> 1, hp = cw & 1;
  const int r = lane & 31, h = lane >> 5, q = (lane & 15) >> 2, p = lane & 3, blk = (lane >> 4) & 1;
  STAMP(0)
  bf16x8 kin[2], qin[2];
  if (wave >= 4) {
    auto rx = __builtin_amdgcn_make_buffer_rsrc((void*)x, 0, 2048 * 1024 * 2, 0x00020000);
    auto rw = __builtin_amdgcn_make_buffer_rsrc((void*)w, 0, 1024 * 3072 * 2, 0x00020000);
    auto rp = __builtin_amdgcn_make_buffer_rsrc((void*)wphi, 0, 32 * 32 * 32 * 2, 0x00020000);
    const unsigned va = (lane >> 3) * 2048 + (((lane & 7) ^ ((4 * (cw & 1) + (lane >> 4)) & 7)) << 4);
    const unsigned vb = (lane >> 3) * 6144 + (((lane & 7) ^ (((lane >> 4) & 1) << 2)) << 4);
    const int cls = (ct - rt) & 3;
    const u64 order = cls == 0 ? 0xfedba9c765832140ull : cls == 1 ? 0xfecbad8769432501ull : cls == 2 ? 0xfdcbe987a5436102ull : 0xedcfa98b65472103ull;
    const int rot = 8 * (b & 1);
    const unsigned sa0 = (rt * 128 + cw * 8) * 2048, sb0 = cw * 8 * 6144 + ct * 128;
    auto issue = [&](int t, int slot) {
      const int kt = ((int)(order >> (4 * t)) + rot) & 15;
      const unsigned dst = lds + slot * STAGE + cw * 1024, sa = sa0 + kt * 128, sb = sb0 + kt * (64 * 6144);
#pragma unroll
      for (int j = 0; j < 4; j++) __builtin_amdgcn_raw_ptr_buffer_load_lds(rx, (LDS3 void*)(size_t)(dst + j * 4096), 16, va, sa + j * (32 * 2048), 0, 0);
#pragma unroll
      for (int j = 0; j < 6; j++) __builtin_amdgcn_raw_ptr_buffer_load_lds(rw, (LDS3 void*)(size_t)(dst + BOFF + j * 4096), 16, vb, sb + (j >> 1) * 2048 + (j & 1) * (32 * 6144), 0, 0);
    };
    issue(0, 0);
    __builtin_amdgcn_raw_ptr_buffer_load_lds(rp, (LDS3 void*)(size_t)(lds + WPHI_OFF + cw * 1024), 16, lane * 16, ct * 4096 + cw * 1024, 0, 0);
    issue(1, 1);
    waitv<11>(); bar();
    issue(2, 2);
    int slot = 0;
#pragma unroll 1
    for (int t = 0; t < 13; t++) {
      waitv<10>(); bar();
      issue(t + 3, slot);
      slot = slot == NS - 1 ? 0 : slot + 1;
    }
    waitv<10>(); bar();
    waitv<0>(); bar();
    bar();
    bar();
    kin[0] = ld128(lds + HQ_OFF + cw * 4096 + lane * 16);
    kin[1] = ld128(lds + HQ_OFF + cw * 4096 + 1024 + lane * 16);
    qin[0] = ld128(lds + HQ_OFF + cw * 4096 + 2048 + lane * 16);
    qin[1] = ld128(lds + HQ_OFF + cw * 4096 + 3072 + lane * 16);
  } else {
    unsigned aa[4];
#pragma unroll
    for (int s = 0; s < 4; s++) aa[s] = lds + (wr * 64 + r) * 128 + (((2 * s + h) ^ ((r >> 1) & 7)) << 4);
    const unsigned bb = lds + BOFF + (8 * h + q) * 128 + (((hp * 4 + blk * 2 + (p >> 1)) ^ (((q >> 1) & 1) << 2)) << 4) + (p & 1) * 8;
    Acc c = {};
    Frag f0, f1;
    bar();
    STAMP(1)
    load_frag<0>(f0, aa[0], bb);
    unsigned so = 0;
#pragma unroll 1
    for (int t = 0; t < 16; t++) {
      SCHED0();
      load_frag<1>(f1, aa[1] + so, bb + so); mma6(c, f0); interleave(); SCHED0();
      load_frag<2>(f0, aa[2] + so, bb + so); mma6(c, f1); interleave(); SCHED0();
      load_frag<3>(f1, aa[3] + so, bb + so); mma6(c, f0); interleave(); SCHED0();
      so = so == (NS - 1) * STAGE ? 0 : so + STAGE;
      lgkm0_bar(); SCHED0();
      load_frag<0>(f0, aa[0] + so, bb + so); mma6(c, f1); interleave(); SCHED0();
    }
    STAMP(2)
    asm volatile("s_waitcnt lgkmcnt(0)" ::: "memory");
    kin[0] = pack8<0>(c.k0); kin[1] = pack8<1>(c.k0);
    const unsigned hq = lds + HQ_OFF + cw * 4096 + lane * 16, vf = lds + VF_OFF + (hp * 4 + wr * 2) * 2048 + lane * 16;
    st128(hq, pack8<0>(c.k1)); st128(hq + 1024, pack8<1>(c.k1));
    st128(hq + 2048, pack8<0>(c.q1)); st128(hq + 3072, pack8<1>(c.q1));
    st128(vf, pack8<0>(c.v0)); st128(vf + 1024, pack8<1>(c.v0));
    st128(vf + 2048, pack8<0>(c.v1)); st128(vf + 3072, pack8<1>(c.v1));
    qin[0] = pack8<0>(c.q0); qin[1] = pack8<1>(c.q0);
    lgkm0_bar();
  }
  const int rb = wr * 2 + (wave >> 2);
  bf16x8 wf[2];
  { const unsigned a = lds + WPHI_OFF + hp * 2048 + (4 * h + q) * 64 + blk * 32 + p * 8;
    wf[0] = tr2(a, a + 512); wf[1] = tr2(a + 1024, a + 1536); }
  bf16x8 P[4];
  feature_map(wf, kin, P);
  { const int sg = (((r >> 1) & 1) << 3) | (r & 1) | (((r >> 2) & 3) << 1);
    const unsigned a0 = lds + PK_OFF + hp * 16384 + (rb * 32 + r) * 128 + ((sg ^ h) << 3);
#pragma unroll
    for (int j = 0; j < 8; j++) {
      const u32x4 v = __builtin_bit_cast(u32x4, P[j >> 1]);
      u32x2 hv; hv.x = v[2 * (j & 1)]; hv.y = v[2 * (j & 1) + 1];
      *(LDS3 u32x2*)(size_t)(a0 ^ (j << 4)) = hv;
    } }
  lgkm0_bar();
  STAMP(3)
  {
    const int hp2 = cw >> 1, fb = cw & 1;
    unsigned ta[2];
#pragma unroll
    for (int t = 0; t < 2; t++) {
      const int s = 8 * t + 4 * h + q, sg = (((s >> 1) & 1) << 3) | (s & 1) | (((s >> 2) & 3) << 1);
      ta[t] = lds + PK_OFF + hp2 * 16384 + s * 128 + (((fb * 8 + blk * 4 + p) ^ sg) << 3);
    }
    const size_t slot = ((size_t)(ct * 16 + rt) * 4 + cw);
    if (wave < 4) {
      f32x16 kv = {};
#pragma unroll
      for (int ks = 0; ks < 8; ks++) kv = MFMA(tr2(ta[0] + ks * 2048, ta[1] + ks * 2048), ld128(lds + VF_OFF + hp2 * 8192 + ks * 1024 + lane * 16), kv);
      f32x4* dst = (f32x4*)(kvp + slot * 1024) + lane;
#pragma unroll
      for (int g = 0; g < 4; g++) dst[g * 64] = (f32x4){kv[4 * g], kv[4 * g + 1], kv[4 * g + 2], kv[4 * g + 3]};
    } else {
      float z0 = 0.f, z1 = 0.f;
      const bf16x2 one = {(__bf16)1.0f, (__bf16)1.0f};
#pragma unroll
      for (int ks = 0; ks < 8; ks++) {
        const bf16x4 u0 = trd(ta[0] + ks * 2048), u1 = trd(ta[1] + ks * 2048);
        z0 = __builtin_amdgcn_fdot2_f32_bf16(__builtin_shufflevector(u0, u0, 0, 1), one, z0, false);
        z1 = __builtin_amdgcn_fdot2_f32_bf16(__builtin_shufflevector(u0, u0, 2, 3), one, z1, false);
        z0 = __builtin_amdgcn_fdot2_f32_bf16(__builtin_shufflevector(u1, u1, 0, 1), one, z0, false);
        z1 = __builtin_amdgcn_fdot2_f32_bf16(__builtin_shufflevector(u1, u1, 2, 3), one, z1, false);
      }
      const float z = xhalf_add(z0 + z1);
      if (lane < 32) zp[slot * 32 + lane] = z;
    }
  }
  STAMP(4)
  feature_map(wf, qin, P);
  u32x4* pq = phiq + ((size_t)((rt * 16 + ct) * 8 + rb * 2 + hp) * 4) * 64 + lane;
#pragma unroll
  for (int j = 0; j < 4; j++) pq[j * 64] = __builtin_bit_cast(u32x4, P[j]);
  STAMP(5)
}

__global__ __launch_bounds__(512, 2) void k2(const u32x4* __restrict__ phiq, const float* __restrict__ kvp, const float* __restrict__ zp, u32x4* __restrict__ ofr) {
  __shared__ __attribute__((aligned(16))) float red[4096 + 128];
  const int b = blockIdx.x; int rt, ct; tile_of_block(b, rt, ct);
  const int tid = threadIdx.x, wave = __builtin_amdgcn_readfirstlane(tid >> 6), lane = tid & 63, h = lane >> 5;
  const int rb = wave >> 1, hp = wave & 1;
  bf16x8 P[4];
  const u32x4* pq = phiq + ((size_t)((rt * 16 + ct) * 8 + wave) * 4) * 64 + lane;
#pragma unroll
  for (int j = 0; j < 4; j++) P[j] = __builtin_bit_cast(bf16x8, pq[j * 64]);
  f32x4 a0 = {}, a1 = {}; float z = 0.f;
#pragma unroll
  for (int j = 0; j < 16; j++) {
    const int jj = (j + rt) & 15;
    const f32x4* src = (const f32x4*)(kvp + (size_t)(ct * 16 + jj) * 4096);
    a0 += src[tid]; a1 += src[512 + tid];
    if (tid < 128) z += zp[(ct * 16 + jj) * 128 + tid];
  }
  ((f32x4*)red)[tid] = a0; ((f32x4*)red)[512 + tid] = a1;
  if (tid < 128) red[4096 + tid] = z;
  __syncthreads();
  f32x16 num = {}, den = {};
#pragma unroll
  for (int j = 0; j < 4; j++) {
    const f32x4 u0 = ((const f32x4*)red)[(hp * 2 + (j >> 1)) * 256 + (2 * (j & 1)) * 64 + lane], u1 = ((const f32x4*)red)[(hp * 2 + (j >> 1)) * 256 + (2 * (j & 1) + 1) * 64 + lane];
    const f32x4 y0 = *(const f32x4*)(red + 4096 + hp * 64 + j * 16 + 4 * h), y1 = *(const f32x4*)(red + 4096 + hp * 64 + j * 16 + 8 + 4 * h);
    const u32x4 ka = {pk2(u0[0], u0[1]), pk2(u0[2], u0[3]), pk2(u1[0], u1[1]), pk2(u1[2], u1[3])};
    const u32x4 za = {pk2(y0[0], y0[1]), pk2(y0[2], y0[3]), pk2(y1[0], y1[1]), pk2(y1[2], y1[3])};
    num = MFMA(__builtin_bit_cast(bf16x8, ka), P[j], num);
    den = MFMA(__builtin_bit_cast(bf16x8, za), P[j], den);
  }
  const float rd = __builtin_amdgcn_rcpf(den[0] + 1e-6f);
#pragma unroll
  for (int i = 0; i < 16; i++) num[i] *= rd;
  u32x4* dst = ofr + ((size_t)((rt * 4 + rb) * 32 + ct * 2 + hp) * 2) * 64 + lane;
  dst[0] = __builtin_bit_cast(u32x4, pack8<0>(num));
  dst[64] = __builtin_bit_cast(u32x4, pack8<1>(num));
}

constexpr int K3_LDS = 131072;
__global__ __launch_bounds__(512, 2) void k3(const u32x4* __restrict__ ofr, const u16* __restrict__ wo, u16* __restrict__ out) {
  extern __shared__ __attribute__((aligned(16))) char smem[];
  const unsigned lds = (unsigned)(size_t)(LDS3 char*)smem;
  const int b = blockIdx.x, xcd = b & 7, i = b >> 3;
  const int rt = (xcd & 3) * 4 + (i & 3), ct = (xcd >> 2) * 8 + (i >> 2);
  const int wave = __builtin_amdgcn_readfirstlane(threadIdx.x >> 6), lane = threadIdx.x & 63;
  const int rb = wave & 3, kh = wave >> 2;
  const int r = lane & 31, h = lane >> 5, q = (lane & 15) >> 2, p = lane & 3, blk = (lane >> 4) & 1;
  auto rw = __builtin_amdgcn_make_buffer_rsrc((void*)wo, 0, 1024 * 1024 * 2, 0x00020000);
  const unsigned vb = (lane >> 3) * 2048 + (((lane & 7) ^ (((lane >> 4) & 1) << 2)) << 4);
#pragma unroll
  for (int j = 0; j < 16; j++) __builtin_amdgcn_raw_ptr_buffer_load_lds(rw, (LDS3 void*)(size_t)(lds + (j * 8 + wave) * 1024), 16, vb, (j * 8 + wave) * 8 * 2048 + ct * 128, 0, 0);
  bf16x8 a[32];
  auto ro = __builtin_amdgcn_make_buffer_rsrc((void*)ofr, 0, 2048 * 1024 * 2, 0x00020000);
#pragma unroll
  for (int j = 0; j < 32; j++) a[j] = __builtin_bit_cast(bf16x8, __builtin_amdgcn_raw_buffer_load_b128(ro, lane * 16, ((rt * 4 + rb) * 64 + kh * 32 + j) * 1024, 0));
  waitv<32>(); bar();
  const int ps = ((p & 1) << 1) | (p >> 1);
  unsigned bb[2];
#pragma unroll
  for (int n = 0; n < 2; n++) bb[n] = lds + kh * 65536 + (4 * h + q) * 128 + (((n * 4 + blk * 2 + (ps >> 1)) ^ (((q >> 1) & 1) << 2)) << 4) + (ps & 1) * 8;
  f32x16 c0 = {}, c1 = {};
#pragma unroll
  for (int j = 0; j < 32; j++) {
    c0 = MFMA(tr2(bb[0] + j * 2048, bb[0] + j * 2048 + 1024), a[j], c0);
    c1 = MFMA(tr2(bb[1] + j * 2048, bb[1] + j * 2048 + 1024), a[j], c1);
  }
  lgkm0_bar();
  { const f32x16 snd = kh ? c0 : c1;
    const unsigned wa = lds + wave * 4096 + lane * 16;
#pragma unroll
    for (int g = 0; g < 4; g++) *(LDS3 f32x4*)(size_t)(wa + g * 1024) = (f32x4){snd[4 * g], snd[4 * g + 1], snd[4 * g + 2], snd[4 * g + 3]}; }
  lgkm0_bar();
  f32x16 c = kh ? c1 : c0;
  { const unsigned ra = lds + (wave ^ 4) * 4096 + lane * 16;
#pragma unroll
    for (int g = 0; g < 4; g++) { const f32x4 v = *(LDS3 const f32x4*)(size_t)(ra + g * 1024); c[4 * g] += v[0]; c[4 * g + 1] += v[1]; c[4 * g + 2] += v[2]; c[4 * g + 3] += v[3]; } }
  u16* dst = out + (size_t)(rt * 128 + rb * 32 + r) * 1024 + ct * 64 + kh * 32 + h * 8;
  *(u32x4*)dst = __builtin_bit_cast(u32x4, pack8<0>(c));
  *(u32x4*)(dst + 16) = __builtin_bit_cast(u32x4, pack8<1>(c));
}

extern "C" void kernel_launch(void* const* d_in, const int* in_sizes, int n_in, void* d_out, int out_size, void* d_ws, size_t ws_size, hipStream_t stream) {
  char* ws = (char*)d_ws;
  u32x4* phiq = (u32x4*)ws;
  float* kvp = (float*)(ws + (8 << 20));
  float* zp = (float*)(ws + (12 << 20));
  u32x4* ofr = (u32x4*)(ws + (13 << 20));
  u64* stamps = (u64*)(ws + (64 << 20));
  (void)hipFuncSetAttribute((const void*)k1, hipFuncAttributeMaxDynamicSharedMemorySize, K1_LDS);
  (void)hipFuncSetAttribute((const void*)k3, hipFuncAttributeMaxDynamicSharedMemorySize, K3_LDS);
  k1<<<256, 512, K1_LDS, stream>>>((const u16*)d_in[0], (const u16*)d_in[1], (const u16*)d_in[2], phiq, kvp, zp, stamps);
  k2<<<256, 512, 0, stream>>>(phiq, kvp, zp, ofr);
  k3<<<256, 512, K3_LDS, stream>>>(ofr, (const u16*)d_in[3], (u16*)d_out);
}
```

```cpp
#include <hip/hip_runtime.h>
#include <hip/hip_bf16.h>
#include <stdint.h>

typedef unsigned short u16;
typedef unsigned long long u64;
typedef __bf16 bf16x2 __attribute__((ext_vector_type(2)));
typedef __bf16 bf16x4 __attribute__((ext_vector_type(4)));
typedef __bf16 bf16x8 __attribute__((ext_vector_type(8)));
typedef float f32x2 __attribute__((ext_vector_type(2)));
typedef float f32x4 __attribute__((ext_vector_type(4)));
typedef float f32x16 __attribute__((ext_vector_type(16)));
typedef unsigned u32x2 __attribute__((ext_vector_type(2)));
typedef unsigned u32x4 __attribute__((ext_vector_type(4)));
#define LDS3 __attribute__((address_space(3)))
#define DI __device__ __forceinline__
#define MFMA(a, b, c) __builtin_amdgcn_mfma_f32_32x32x16_bf16((a), (b), (c), 0, 0, 0)
#define SCHED0() __builtin_amdgcn_sched_barrier(0)
#define STAMP(i)

template <int N> DI void waitv() { asm volatile("s_waitcnt vmcnt(%0)" ::"n"(N) : "memory"); }
DI void bar() { asm volatile("s_barrier" ::: "memory"); }
DI void lgkm0_bar() { asm volatile("s_waitcnt lgkmcnt(0)\n\ts_barrier" ::: "memory"); }
DI bf16x8 ld128(unsigned a) { return *(LDS3 const bf16x8*)(size_t)a; }
DI void st128(unsigned a, bf16x8 v) { *(LDS3 bf16x8*)(size_t)a = v; }
DI bf16x4 trd(unsigned a) { return __builtin_amdgcn_ds_read_tr16_b64_v4bf16((LDS3 bf16x4*)(size_t)a); }
DI bf16x8 tr2(unsigned a0, unsigned a1) { return __builtin_shufflevector(trd(a0), trd(a1), 0, 1, 2, 3, 4, 5, 6, 7); }
DI unsigned pk2(float lo, float hi) { f32x2 t = {lo, hi}; return __builtin_bit_cast(unsigned, __builtin_convertvector(t, bf16x2)); }
template <int S> DI bf16x8 pack8(const f32x16& x) {
  u32x4 p = {pk2(x[8 * S], x[8 * S + 1]), pk2(x[8 * S + 2], x[8 * S + 3]), pk2(x[8 * S + 4], x[8 * S + 5]), pk2(x[8 * S + 6], x[8 * S + 7])};
  return __builtin_bit_cast(bf16x8, p);
}
DI float xhalf_max(float v) { auto r = __builtin_amdgcn_permlane32_swap(__float_as_uint(v), __float_as_uint(v), false, false); return __builtin_fmaxf(__uint_as_float(r[0]), __uint_as_float(r[1])); }
DI float xhalf_min(float v) { auto r = __builtin_amdgcn_permlane32_swap(__float_as_uint(v), __float_as_uint(v), false, false); return __builtin_fminf(__uint_as_float(r[0]), __uint_as_float(r[1])); }
DI float xhalf_add(float v) { auto r = __builtin_amdgcn_permlane32_swap(__float_as_uint(v), __float_as_uint(v), false, false); return __uint_as_float(r[0]) + __uint_as_float(r[1]); }

constexpr int NS = 3, STAGE = 40960, BOFF = 16384, WPHI_OFF = NS * STAGE;
constexpr int K1_LDS = WPHI_OFF + 4096;
constexpr int HQ_OFF = 0, VF_OFF = 16384, PK_OFF = 32768;

DI void feature_map(const bf16x8 (&wf)[2], const bf16x8 (&in)[2], bf16x8 (&P)[4]) {
  f32x16 p = {};
  p = MFMA(wf[0], in[0], p);
  p = MFMA(wf[1], in[1], p);
  float mx = p[0], mn = p[0];
#pragma unroll
  for (int i = 1; i < 16; i++) { mx = __builtin_fmaxf(mx, p[i]); mn = __builtin_fminf(mn, p[i]); }
  mx = xhalf_max(mx); mn = xhalf_min(mn);
  const float c = 1.4426950408889634f;
  const float a = -mx * c, b = mn * c;
  f32x16 e, g; float s0 = 0.f, s1 = 0.f, t0 = 0.f, t1 = 0.f;
#pragma unroll
  for (int i = 0; i < 16; i++) { e[i] = __builtin_amdgcn_exp2f(__builtin_fmaf(p[i], c, a)); g[i] = __builtin_amdgcn_exp2f(__builtin_fmaf(p[i], -c, b)); }
#pragma unroll
  for (int i = 0; i < 16; i += 2) { s0 += e[i]; s1 += e[i + 1]; t0 += g[i]; t1 += g[i + 1]; }
  const float rs = __builtin_amdgcn_rcpf(xhalf_add(s0 + s1)), rt = __builtin_amdgcn_rcpf(xhalf_add(t0 + t1));
#pragma unroll
  for (int i = 0; i < 16; i++) { e[i] *= rs; g[i] *= rt; }
  P[0] = pack8<0>(e); P[1] = pack8<1>(e); P[2] = pack8<0>(g); P[3] = pack8<1>(g);
}

struct Frag { bf16x8 x0, x1, wq, wk, wv; };
template <int S> DI void load_frag(Frag& f, unsigned a, unsigned b) {
  f.x0 = ld128(a);
  f.wq = tr2(b + S * 2048, b + S * 2048 + 512);
  f.wk = tr2(b + 8192 + S * 2048, b + 8192 + S * 2048 + 512);
  f.wv = tr2(b + 16384 + S * 2048, b + 16384 + S * 2048 + 512);
  f.x1 = ld128(a + 4096);
}
struct Acc { f32x16 q0, q1, k0, k1, v0, v1; };
DI void mma6(Acc& c, const Frag& f) {
  c.q0 = MFMA(f.wq, f.x0, c.q0);
  c.k0 = MFMA(f.wk, f.x0, c.k0);
  c.v0 = MFMA(f.x0, f.wv, c.v0);
  c.q1 = MFMA(f.wq, f.x1, c.q1);
  c.k1 = MFMA(f.wk, f.x1, c.k1);
  c.v1 = MFMA(f.x1, f.wv, c.v1);
}
DI void interleave() {
  __builtin_amdgcn_sched_group_barrier(0x8, 1, 0); __builtin_amdgcn_sched_group_barrier(0x100, 2, 0);
  __builtin_amdgcn_sched_group_barrier(0x8, 1, 0); __builtin_amdgcn_sched_group_barrier(0x100, 2, 0);
  __builtin_amdgcn_sched_group_barrier(0x8, 1, 0); __builtin_amdgcn_sched_group_barrier(0x100, 2, 0);
  __builtin_amdgcn_sched_group_barrier(0x8, 1, 0); __builtin_amdgcn_sched_group_barrier(0x100, 2, 0);
  __builtin_amdgcn_sched_group_barrier(0x8, 2, 0);
}

DI void tile_of_block(int b, int& rt, int& ct) {
  const int xcd = b & 7, i = b >> 3;
  rt = (xcd & 1) * 8 + (i & 7); ct = (xcd >> 1) * 4 + (i >> 3);
}

__global__ __launch_bounds__(512, 2) void k1(const u16* __restrict__ x, const u16* __restrict__ w, const u16* __restrict__ wphi,
                                             u32x4* __restrict__ phiq, float* __restrict__ kvp, float* __restrict__ zp, u64* stamps) {
  extern __shared__ __attribute__((aligned(16))) char smem[];
  const unsigned lds = (unsigned)(size_t)(LDS3 char*)smem;
  const int b = blockIdx.x; int rt, ct; tile_of_block(b, rt, ct);
  const int wave = __builtin_amdgcn_readfirstlane(threadIdx.x >> 6), lane = threadIdx.x & 63;
  const int cw = wave & 3, wr = cw >> 1, hp = cw & 1;
  const int r = lane & 31, h = lane >> 5, q = (lane & 15) >> 2, p = lane & 3, blk = (lane >> 4) & 1;
  STAMP(0)
  bf16x8 kin[2], qin[2];
  if (wave >= 4) {
    auto rx = __builtin_amdgcn_make_buffer_rsrc((void*)x, 0, 2048 * 1024 * 2, 0x00020000);
    auto rw = __builtin_amdgcn_make_buffer_rsrc((void*)w, 0, 1024 * 3072 * 2, 0x00020000);
    auto rp = __builtin_amdgcn_make_buffer_rsrc((void*)wphi, 0, 32 * 32 * 32 * 2, 0x00020000);
    const unsigned va = (lane >> 3) * 2048 + (((lane & 7) ^ ((4 * (cw & 1) + (lane >> 4)) & 7)) << 4);
    const unsigned vb = (lane >> 3) * 6144 + (((lane & 7) ^ (((lane >> 4) & 1) << 2)) << 4);
    const int cls = (ct - rt) & 3;
    const u64 order = cls == 0 ? 0xfedba9c765832140ull : cls == 1 ? 0xfecbad8769432501ull : cls == 2 ? 0xfdcbe987a5436102ull : 0xedcfa98b65472103ull;
    const int rot = 8 * (b & 1);
    const unsigned sa0 = (rt * 128 + cw * 8) * 2048, sb0 = cw * 8 * 6144 + ct * 128;
    auto issue = [&](int t, int slot) {
      const int kt = ((int)(order >> (4 * t)) + rot) & 15;
      const unsigned dst = lds + slot * STAGE + cw * 1024, sa = sa0 + kt * 128, sb = sb0 + kt * (64 * 6144);
#pragma unroll
      for (int j = 0; j < 4; j++) __builtin_amdgcn_raw_ptr_buffer_load_lds(rx, (LDS3 void*)(size_t)(dst + j * 4096), 16, va, sa + j * (32 * 2048), 0, 0);
#pragma unroll
      for (int j = 0; j < 6; j++) __builtin_amdgcn_raw_ptr_buffer_load_lds(rw, (LDS3 void*)(size_t)(dst + BOFF + j * 4096), 16, vb, sb + (j >> 1) * 2048 + (j & 1) * (32 * 6144), 0, 0);
    };
    issue(0, 0);
    __builtin_amdgcn_raw_ptr_buffer_load_lds(rp, (LDS3 void*)(size_t)(lds + WPHI_OFF + cw * 1024), 16, lane * 16, ct * 4096 + cw * 1024, 0, 0);
    issue(1, 1);
    waitv<11>(); bar();
    issue(2, 2);
    int slot = 0;
#pragma unroll 1
    for (int t = 0; t < 13; t++) {
      waitv<10>(); bar();
      issue(t + 3, slot);
      slot = slot == NS - 1 ? 0 : slot + 1;
    }
    waitv<10>(); bar();
    waitv<0>(); bar();
    bar();
    bar();
    kin[0] = ld128(lds + HQ_OFF + cw * 4096 + lane * 16);
    kin[1] = ld128(lds + HQ_OFF + cw * 4096 + 1024 + lane * 16);
    qin[0] = ld128(lds + HQ_OFF + cw * 4096 + 2048 + lane * 16);
    qin[1] = ld128(lds + HQ_OFF + cw * 4096 + 3072 + lane * 16);
  } else {
    unsigned aa[4];
#pragma unroll
    for (int s = 0; s < 4; s++) aa[s] = lds + (wr * 64 + r) * 128 + (((2 * s + h) ^ ((r >> 1) & 7)) << 4);
    const unsigned bb = lds + BOFF + (8 * h + q) * 128 + (((hp * 4 + blk * 2 + (p >> 1)) ^ (((q >> 1) & 1) << 2)) << 4) + (p & 1) * 8;
    Acc c = {};
    Frag f0, f1;
    bar();
    STAMP(1)
    load_frag<0>(f0, aa[0], bb);
    unsigned so = 0;
#pragma unroll 1
    for (int t = 0; t < 16; t++) {
      SCHED0();
      load_frag<1>(f1, aa[1] + so, bb + so); mma6(c, f0); interleave(); SCHED0();
      load_frag<2>(f0, aa[2] + so, bb + so); mma6(c, f1); interleave(); SCHED0();
      load_frag<3>(f1, aa[3] + so, bb + so); mma6(c, f0); interleave(); SCHED0();
      so = so == (NS - 1) * STAGE ? 0 : so + STAGE;
      lgkm0_bar(); SCHED0();
      load_frag<0>(f0, aa[0] + so, bb + so); mma6(c, f1); interleave(); SCHED0();
    }
    STAMP(2)
    asm volatile("s_waitcnt lgkmcnt(0)" ::: "memory");
    kin[0] = pack8<0>(c.k0); kin[1] = pack8<1>(c.k0);
    const unsigned hq = lds + HQ_OFF + cw * 4096 + lane * 16, vf = lds + VF_OFF + (hp * 4 + wr * 2) * 2048 + lane * 16;
    st128(hq, pack8<0>(c.k1)); st128(hq + 1024, pack8<1>(c.k1));
    st128(hq + 2048, pack8<0>(c.q1)); st128(hq + 3072, pack8<1>(c.q1));
    st128(vf, pack8<0>(c.v0)); st128(vf + 1024, pack8<1>(c.v0));
    st128(vf + 2048, pack8<0>(c.v1)); st128(vf + 3072, pack8<1>(c.v1));
    qin[0] = pack8<0>(c.q0); qin[1] = pack8<1>(c.q0);
    lgkm0_bar();
  }
  const int rb = wr * 2 + (wave >> 2);
  bf16x8 wf[2];
  { const unsigned a = lds + WPHI_OFF + hp * 2048 + (4 * h + q) * 64 + blk * 32 + p * 8;
    wf[0] = tr2(a, a + 512); wf[1] = tr2(a + 1024, a + 1536); }
  bf16x8 P[4];
  feature_map(wf, kin, P);
  { const int sg = (((r >> 1) & 1) << 3) | (r & 1) | (((r >> 2) & 3) << 1);
    const unsigned a0 = lds + PK_OFF + hp * 16384 + (rb * 32 + r) * 128 + ((sg ^ h) << 3);
#pragma unroll
    for (int j = 0; j < 8; j++) {
      const u32x4 v = __builtin_bit_cast(u32x4, P[j >> 1]);
      u32x2 hv; hv.x = v[2 * (j & 1)]; hv.y = v[2 * (j & 1) + 1];
      *(LDS3 u32x2*)(size_t)(a0 ^ (j << 4)) = hv;
    } }
  lgkm0_bar();
  STAMP(3)
  {
    const int hp2 = cw >> 1, fb = cw & 1;
    unsigned ta[2];
#pragma unroll
    for (int t = 0; t < 2; t++) {
      const int s = 8 * t + 4 * h + q, sg = (((s >> 1) & 1) << 3) | (s & 1) | (((s >> 2) & 3) << 1);
      ta[t] = lds + PK_OFF + hp2 * 16384 + s * 128 + (((fb * 8 + blk * 4 + p) ^ sg) << 3);
    }
    const size_t slot = ((size_t)(ct * 16 + rt) * 4 + cw);
    if (wave < 4) {
      f32x16 kv = {};
#pragma unroll
      for (int ks = 0; ks < 8; ks++) kv = MFMA(tr2(ta[0] + ks * 2048, ta[1] + ks * 2048), ld128(lds + VF_OFF + hp2 * 8192 + ks * 1024 + lane * 16), kv);
      f32x4* dst = (f32x4*)(kvp + slot * 1024) + lane;
#pragma unroll
      for (int g = 0; g < 4; g++) dst[g * 64] = (f32x4){kv[4 * g], kv[4 * g + 1], kv[4 * g + 2], kv[4 * g + 3]};
    } else {
      float z0 = 0.f, z1 = 0.f;
      const bf16x2 one = {(__bf16)1.0f, (__bf16)1.0f};
#pragma unroll
      for (int ks = 0; ks < 8; ks++) {
        const bf16x4 u0 = trd(ta[0] + ks * 2048), u1 = trd(ta[1] + ks * 2048);
        z0 = __builtin_amdgcn_fdot2_f32_bf16(__builtin_shufflevector(u0, u0, 0, 1), one, z0, false);
        z1 = __builtin_amdgcn_fdot2_f32_bf16(__builtin_shufflevector(u0, u0, 2, 3), one, z1, false);
        z0 = __builtin_amdgcn_fdot2_f32_bf16(__builtin_shufflevector(u1, u1, 0, 1), one, z0, false);
        z1 = __builtin_amdgcn_fdot2_f32_bf16(__builtin_shufflevector(u1, u1, 2, 3), one, z1, false);
      }
      const float z = xhalf_add(z0 + z1);
      if (lane < 32) zp[slot * 32 + lane] = z;
    }
  }
  STAMP(4)
  feature_map(wf, qin, P);
  u32x4* pq = phiq + ((size_t)((rt * 16 + ct) * 8 + rb * 2 + hp) * 4) * 64 + lane;
#pragma unroll
  for (int j = 0; j < 4; j++) pq[j * 64] = __builtin_bit_cast(u32x4, P[j]);
  STAMP(5)
}

__global__ __launch_bounds__(512, 2) void k2(const u32x4* __restrict__ phiq, const float* __restrict__ kvp, const float* __restrict__ zp, u32x4* __restrict__ ofr) {
  __shared__ __attribute__((aligned(16))) float red[4096 + 128];
  const int b = blockIdx.x; int rt, ct; tile_of_block(b, rt, ct);
  const int tid = threadIdx.x, wave = __builtin_amdgcn_readfirstlane(tid >> 6), lane = tid & 63, h = lane >> 5;
  const int rb = wave >> 1, hp = wave & 1;
  bf16x8 P[4];
  const u32x4* pq = phiq + ((size_t)((rt * 16 + ct) * 8 + wave) * 4) * 64 + lane;
#pragma unroll
  for (int j = 0; j < 4; j++) P[j] = __builtin_bit_cast(bf16x8, pq[j * 64]);
  f32x4 a0 = {}, a1 = {}; float z = 0.f;
#pragma unroll
  for (int j = 0; j < 16; j++) {
    const int jj = (j + rt) & 15;
    const f32x4* src = (const f32x4*)(kvp + (size_t)(ct * 16 + jj) * 4096);
    a0 += src[tid]; a1 += src[512 + tid];
    if (tid < 128) z += zp[(ct * 16 + jj) * 128 + tid];
  }
  ((f32x4*)red)[tid] = a0; ((f32x4*)red)[512 + tid] = a1;
  if (tid < 128) red[4096 + tid] = z;
  __syncthreads();
  f32x16 num = {}, den = {};
#pragma unroll
  for (int j = 0; j < 4; j++) {
    const f32x4 u0 = ((const f32x4*)red)[(hp * 2 + (j >> 1)) * 256 + (2 * (j & 1)) * 64 + lane], u1 = ((const f32x4*)red)[(hp * 2 + (j >> 1)) * 256 + (2 * (j & 1) + 1) * 64 + lane];
    const f32x4 y0 = *(const f32x4*)(red + 4096 + hp * 64 + j * 16 + 4 * h), y1 = *(const f32x4*)(red + 4096 + hp * 64 + j * 16 + 8 + 4 * h);
    const u32x4 ka = {pk2(u0[0], u0[1]), pk2(u0[2], u0[3]), pk2(u1[0], u1[1]), pk2(u1[2], u1[3])};
    const u32x4 za = {pk2(y0[0], y0[1]), pk2(y0[2], y0[3]), pk2(y1[0], y1[1]), pk2(y1[2], y1[3])};
    num = MFMA(__builtin_bit_cast(bf16x8, ka), P[j], num);
    den = MFMA(__builtin_bit_cast(bf16x8, za), P[j], den);
  }
  const float rd = __builtin_amdgcn_rcpf(den[0] + 1e-6f);
#pragma unroll
  for (int i = 0; i < 16; i++) num[i] *= rd;
  u32x4* dst = ofr + ((size_t)((rt * 4 + rb) * 32 + ct * 2 + hp) * 2) * 64 + lane;
  dst[0] = __builtin_bit_cast(u32x4, pack8<0>(num));
  dst[64] = __builtin_bit_cast(u32x4, pack8<1>(num));
}

constexpr int K3_LDS = 131072;
__global__ __launch_bounds__(512, 2) void k3(const u32x4* __restrict__ ofr, const u16* __restrict__ wo, u16* __restrict__ out) {
  extern __shared__ __attribute__((aligned(16))) char smem[];
  const unsigned lds = (unsigned)(size_t)(LDS3 char*)smem;
  const int b = blockIdx.x, xcd = b & 7, i = b >> 3;
  const int rt = (xcd & 3) * 4 + (i & 3), ct = (xcd >> 2) * 8 + (i >> 2);
  const int wave = __builtin_amdgcn_readfirstlane(threadIdx.x >> 6), lane = threadIdx.x & 63;
  const int rb = wave & 3, kh = wave >> 2;
  const int r = lane & 31, h = lane >> 5, q = (lane & 15) >> 2, p = lane & 3, blk = (lane >> 4) & 1;
  auto rw = __builtin_amdgcn_make_buffer_rsrc((void*)wo, 0, 1024 * 1024 * 2, 0x00020000);
  const unsigned vb = (lane >> 3) * 2048 + (((lane & 7) ^ (((lane >> 4) & 1) << 2)) << 4);
#pragma unroll
  for (int j = 0; j < 16; j++) __builtin_amdgcn_raw_ptr_buffer_load_lds(rw, (LDS3 void*)(size_t)(lds + (j * 8 + wave) * 1024), 16, vb, (j * 8 + wave) * 8 * 2048 + ct * 128, 0, 0);
  bf16x8 a[32];
  auto ro = __builtin_amdgcn_make_buffer_rsrc((void*)ofr, 0, 2048 * 1024 * 2, 0x00020000);
#pragma unroll
  for (int j = 0; j < 32; j++) a[j] = __builtin_bit_cast(bf16x8, __builtin_amdgcn_raw_buffer_load_b128(ro, lane * 16, ((rt * 4 + rb) * 64 + kh * 32 + j) * 1024, 0));
  waitv<32>(); bar();
  const int ps = ((p & 1) << 1) | (p >> 1);
  unsigned bb[2];
#pragma unroll
  for (int n = 0; n < 2; n++) bb[n] = lds + kh * 65536 + (4 * h + q) * 128 + (((n * 4 + blk * 2 + (ps >> 1)) ^ (((q >> 1) & 1) << 2)) << 4) + (ps & 1) * 8;
  f32x16 c0 = {}, c1 = {};
#pragma unroll
  for (int j = 0; j < 32; j++) {
    c0 = MFMA(tr2(bb[0] + j * 2048, bb[0] + j * 2048 + 1024), a[j], c0);
    c1 = MFMA(tr2(bb[1] + j * 2048, bb[1] + j * 2048 + 1024), a[j], c1);
  }
  lgkm0_bar();
  { const f32x16 snd = kh ? c0 : c1;
    const unsigned wa = lds + wave * 4096 + lane * 16;
#pragma unroll
    for (int g = 0; g < 4; g++) *(LDS3 f32x4*)(size_t)(wa + g * 1024) = (f32x4){snd[4 * g], snd[4 * g + 1], snd[4 * g + 2], snd[4 * g + 3]}; }
  lgkm0_bar();
  f32x16 c = kh ? c1 : c0;
  { const unsigned ra = lds + (wave ^ 4) * 4096 + lane * 16;
#pragma unroll
    for (int g = 0; g < 4; g++) { const f32x4 v = *(LDS3 const f32x4*)(size_t)(ra + g * 1024); c[4 * g] += v[0]; c[4 * g + 1] += v[1]; c[4 * g + 2] += v[2]; c[4 * g + 3] += v[3]; } }
  u16* dst = out + (size_t)(rt * 128 + rb * 32 + r) * 1024 + ct * 64 + kh * 32 + h * 8;
  *(u32x4*)dst = __builtin_bit_cast(u32x4, pack8<0>(c));
  *(u32x4*)(dst + 16) = __builtin_bit_cast(u32x4, pack8<1>(c));
}

#define WHAT 0

#define BLK asm volatile(".rept 64\n\tv_fma_f32 %0, %0, %0, %0\n\t.endr" : "+v"(x));
__global__ __launch_bounds__(512) void probe(u64* out, float* sink){
  extern __shared__ char smem[];
  u64 r0,c0,c1,c2; float x = threadIdx.x;
  asm volatile("s_memrealtime %0\n\ts_memtime %1\n\ts_waitcnt lgkmcnt(0)" : "=&s"(r0), "=&s"(c0) :: "memory");
  asm volatile("s_memtime %0\n\ts_waitcnt lgkmcnt(0)" : "=&s"(c1) : "s"(out) : "memory");
  BLK BLK BLK BLK BLK BLK BLK BLK BLK BLK BLK BLK BLK BLK BLK BLK
  asm volatile("s_memtime %0\n\ts_waitcnt lgkmcnt(0)" : "=&s"(c2) :: "memory");
  if (threadIdx.x==0){ out[blockIdx.x*4]=r0; out[blockIdx.x*4+1]=c1-c0; out[blockIdx.x*4+2]=c2-c1; }
  if (x==0.12345f) sink[0]=x;
}
__global__ void spin(const u64* st){
  const int l=threadIdx.x; u64 mn=~0ull,mx=0,a1=0,a2=0;
  for(int j=0;j<256;j++){ u64 r=st[j*4]; mn=r<mn?r:mn; mx=r>mx?r:mx; a1+=st[j*4+1]; a2+=st[j*4+2]; }
  u64 d = WHAT==0 ? 0 : WHAT==1 ? (mx-mn)*100 : WHAT==2 ? a1/256*10 : WHAT==3 ? a2/256 : (st[(WHAT-4)*4]-mn)*100;
  if (st[5]==0x123456789ull) d++;
  if(d>100000) d=100000;
  u64 t0=__builtin_amdgcn_s_memrealtime();
  while(__builtin_amdgcn_s_memrealtime()-t0<d) __builtin_amdgcn_s_sleep(1);
}
extern "C" void kernel_launch(void* const* d_in, const int* in_sizes, int n_in, void* d_out, int out_size, void* d_ws, size_t ws_size, hipStream_t stream) {
  char* ws = (char*)d_ws;
  u32x4* phiq = (u32x4*)ws;
  float* kvp = (float*)(ws + (8 << 20));
  float* zp = (float*)(ws + (12 << 20));
  u32x4* ofr = (u32x4*)(ws + (13 << 20));
  u64* stamps = (u64*)(ws + (64 << 20));
  (void)hipFuncSetAttribute((const void*)k1, hipFuncAttributeMaxDynamicSharedMemorySize, K1_LDS);
  (void)hipFuncSetAttribute((const void*)k3, hipFuncAttributeMaxDynamicSharedMemorySize, K3_LDS);
  (void)hipFuncSetAttribute((const void*)probe, hipFuncAttributeMaxDynamicSharedMemorySize, 100000);
  probe<<<256,512,100000,stream>>>(stamps,(float*)(ws+(70<<20)));
  spin<<<1,64,0,stream>>>(stamps);
  k1<<<256, 512, K1_LDS, stream>>>((const u16*)d_in[0], (const u16*)d_in[1], (const u16*)d_in[2], phiq, kvp, zp, stamps);
  k2<<<256, 512, 0, stream>>>(phiq, kvp, zp, ofr);
  k3<<<256, 512, K3_LDS, stream>>>(ofr, (const u16*)d_in[3], (u16*)d_out);
}
```

```cpp
#include <hip/hip_runtime.h>
#include <hip/hip_bf16.h>
#include <stdint.h>

typedef unsigned short u16;
typedef unsigned long long u64;
typedef __bf16 bf16x2 __attribute__((ext_vector_type(2)));
typedef __bf16 bf16x4 __attribute__((ext_vector_type(4)));
typedef __bf16 bf16x8 __attribute__((ext_vector_type(8)));
typedef float f32x2 __attribute__((ext_vector_type(2)));
typedef float f32x4 __attribute__((ext_vector_type(4)));
typedef float f32x16 __attribute__((ext_vector_type(16)));
typedef unsigned u32x2 __attribute__((ext_vector_type(2)));
typedef unsigned u32x4 __attribute__((ext_vector_type(4)));
typedef __amdgpu_buffer_rsrc_t rsrc_t;
#define LDS3 __attribute__((address_space(3)))
#define DI __device__ __forceinline__
#define MFMA32(a, b, c) __builtin_amdgcn_mfma_f32_32x32x16_bf16((a), (b), (c), 0, 0, 0)
#define MFMA16(a, b, c) __builtin_amdgcn_mfma_f32_16x16x32_bf16((a), (b), (c), 0, 0, 0)
#define SCHED0() __builtin_amdgcn_sched_barrier(0)
#define STAMP_DECL
#define STAMP(i)
#define STAMP_FLUSH
constexpr int SC1 = 16;

template <int N> DI void waitv() { asm volatile("s_waitcnt vmcnt(%0)" ::"n"(N) : "memory"); }
DI void bar() { asm volatile("s_barrier" ::: "memory"); }
DI void lgkm0_bar() { asm volatile("s_waitcnt lgkmcnt(0)\n\ts_barrier" ::: "memory"); }
DI void all0_bar() { asm volatile("s_waitcnt vmcnt(0) lgkmcnt(0)\n\ts_barrier" ::: "memory"); }
DI bf16x8 ld128(unsigned a) { return *(LDS3 const bf16x8*)(size_t)a; }
DI bf16x4 ld64(unsigned a) { return *(LDS3 const bf16x4*)(size_t)a; }
DI void st128(unsigned a, u32x4 v) { *(LDS3 u32x4*)(size_t)a = v; }
DI bf16x4 trd(unsigned a) { return __builtin_amdgcn_ds_read_tr16_b64_v4bf16((LDS3 bf16x4*)(size_t)a); }
DI bf16x8 cat(bf16x4 a, bf16x4 b) { return __builtin_shufflevector(a, b, 0, 1, 2, 3, 4, 5, 6, 7); }
DI bf16x8 tr2(unsigned a0, unsigned a1) { return cat(trd(a0), trd(a1)); }
DI unsigned pk2(float lo, float hi) { f32x2 t = {lo, hi}; return __builtin_bit_cast(unsigned, __builtin_convertvector(t, bf16x2)); }
DI u32x4 pk44(const f32x4& a, const f32x4& b) { return (u32x4){pk2(a[0], a[1]), pk2(a[2], a[3]), pk2(b[0], b[1]), pk2(b[2], b[3])}; }
template <int S> DI u32x4 pack8(const f32x16& x) {
  return (u32x4){pk2(x[8 * S], x[8 * S + 1]), pk2(x[8 * S + 2], x[8 * S + 3]), pk2(x[8 * S + 4], x[8 * S + 5]), pk2(x[8 * S + 6], x[8 * S + 7])};
}
DI bf16x8 as8(u32x4 v) { return __builtin_bit_cast(bf16x8, v); }
DI float xhalf_max(float v) { auto r = __builtin_amdgcn_permlane32_swap(__float_as_uint(v), __float_as_uint(v), false, false); return __builtin_fmaxf(__uint_as_float(r[0]), __uint_as_float(r[1])); }
DI float xhalf_min(float v) { auto r = __builtin_amdgcn_permlane32_swap(__float_as_uint(v), __float_as_uint(v), false, false); return __builtin_fminf(__uint_as_float(r[0]), __uint_as_float(r[1])); }
DI float xhalf_add(float v) { auto r = __builtin_amdgcn_permlane32_swap(__float_as_uint(v), __float_as_uint(v), false, false); return __uint_as_float(r[0]) + __uint_as_float(r[1]); }
DI void swap16(u32x4& a, u32x4& b) {
#pragma unroll
  for (int i = 0; i < 4; i++) { auto r = __builtin_amdgcn_permlane16_swap(a[i], b[i], false, false); a[i] = r[0]; b[i] = r[1]; }
}
DI void glds(rsrc_t rs, unsigned dst, unsigned voff, unsigned soff) {
  asm volatile("s_mov_b32 m0, %0\n\ts_nop 0\n\tbuffer_load_dwordx4 %1, %2, %3 offen lds" ::"s"(dst), "v"(voff), "s"(rs), "s"(soff) : "memory");
}

constexpr int NS = 3, STAGE = 40960, BOFF = 16384, WPHI_OFF = NS * STAGE;
constexpr int PK_OFF = 0, VF_OFF = 32768;
constexpr int KVF_OFF = 0, ZF_OFF = 49152 + 0;
constexpr int WO_OFF = 32768, LDS_BYTES = 163840;
constexpr unsigned WS_NONCE = 0, WS_MSG = 4096, WS_OFLAG = WS_MSG + 256 * 512, WS_Z = WS_OFLAG + 8192, WS_KVR = WS_Z + 256 * 512,
                   WS_IN = WS_KVR + 16 * 8192, WS_O = WS_IN + 256 * 16384, WS_END = WS_O + 2048 * 1024 * 2;
constexpr unsigned SPIN_CAP = 1u << 18;

DI void feature_map(const bf16x8 (&wf)[2], const u32x4 (&in)[2], u32x4 (&P)[4]) {
  f32x16 p = {};
  p = MFMA32(wf[0], as8(in[0]), p);
  p = MFMA32(wf[1], as8(in[1]), p);
  float mx = p[0], mn = p[0];
#pragma unroll
  for (int i = 1; i < 16; i++) { mx = __builtin_fmaxf(mx, p[i]); mn = __builtin_fminf(mn, p[i]); }
  mx = xhalf_max(mx); mn = xhalf_min(mn);
  const float c = 1.4426950408889634f;
  const float a = -mx * c, b = mn * c;
  f32x16 e, g; float s0 = 0.f, s1 = 0.f, t0 = 0.f, t1 = 0.f;
#pragma unroll
  for (int i = 0; i < 16; i++) { e[i] = __builtin_amdgcn_exp2f(__builtin_fmaf(p[i], c, a)); g[i] = __builtin_amdgcn_exp2f(__builtin_fmaf(p[i], -c, b)); }
#pragma unroll
  for (int i = 0; i < 16; i += 2) { s0 += e[i]; s1 += e[i + 1]; t0 += g[i]; t1 += g[i + 1]; }
  const float rs = __builtin_amdgcn_rcpf(xhalf_add(s0 + s1)), rt = __builtin_amdgcn_rcpf(xhalf_add(t0 + t1));
#pragma unroll
  for (int i = 0; i < 16; i++) { e[i] *= rs; g[i] *= rt; }
  P[0] = pack8<0>(e); P[1] = pack8<1>(e); P[2] = pack8<0>(g); P[3] = pack8<1>(g);
}

struct Frag { bf16x8 x[4], w[6]; };
template <int S> DI void load_frag(Frag& f, unsigned a, unsigned b0, unsigned b1) {
  f.x[0] = ld128(a);
#pragma unroll
  for (int g = 0; g < 3; g++) {
    f.w[2 * g] = tr2(b0 + g * 8192 + S * 4096, b0 + g * 8192 + S * 4096 + 512);
    f.w[2 * g + 1] = tr2(b1 + g * 8192 + S * 4096, b1 + g * 8192 + S * 4096 + 512);
  }
#pragma unroll
  for (int m = 1; m < 4; m++) f.x[m] = ld128(a + m * 2048);
}
DI void mma24(f32x4 (&c)[4][6], const Frag& f) {
#pragma unroll
  for (int m = 0; m < 4; m++)
#pragma unroll
    for (int n = 0; n < 6; n++) {
      if (n < 4) c[m][n] = MFMA16(f.w[n], f.x[m], c[m][n]);
      else c[m][n] = MFMA16(f.x[m], f.w[n], c[m][n]);
    }
}
DI void interleave() {
#pragma unroll
  for (int i = 0; i < 16; i++) { __builtin_amdgcn_sched_group_barrier(0x8, 1, 0); __builtin_amdgcn_sched_group_barrier(0x100, 1, 0); }
  __builtin_amdgcn_sched_group_barrier(0x8, 8, 0);
}

DI int blk_of1(int rt, int ct) { return ((rt & 7) + 8 * (ct & 3)) * 8 + (rt >> 3) + 2 * (ct >> 2); }
DI int blk_of2(int rt, int ct) { return ((rt & 3) + 4 * (ct & 7)) * 8 + (rt >> 2) + 4 * (ct >> 3); }

__global__ __launch_bounds__(512, 2) void hedgehog_fused(const u16* __restrict__ x, const u16* __restrict__ w, const u16* __restrict__ wphi,
                                                         const u16* __restrict__ wo, u16* __restrict__ out, char* __restrict__ ws) {
  extern __shared__ __attribute__((aligned(16))) char smem[];
  STAMP_DECL
  STAMP(0)
  const unsigned lds = (unsigned)(size_t)(LDS3 char*)smem;
  const int b = blockIdx.x, xcd = b & 7, bi = b >> 3;
  const int rt = (xcd & 1) * 8 + (bi & 7), ct = (xcd >> 1) * 4 + (bi >> 3);
  const int rt2 = (xcd & 3) * 4 + (bi & 3), ct2 = (xcd >> 2) * 8 + (bi >> 2);
  const int rot2 = (4 * (bi & 3) + 2 * (bi >> 2)) & 15;
  const int wave = __builtin_amdgcn_readfirstlane(threadIdx.x >> 6), lane = threadIdx.x & 63;
  const int cw = wave & 3, wr = cw >> 1, hp = cw & 1;
  const int r = lane & 31, h = lane >> 5, q = (lane & 15) >> 2, p = lane & 3, blk = (lane >> 4) & 1, G = lane >> 4, l15 = lane & 15;
  asm volatile("" : "+s"(x), "+s"(w), "+s"(wphi), "+s"(wo), "+s"(out), "+s"(ws));
  const rsrc_t rs = __builtin_amdgcn_make_buffer_rsrc(ws, 0, WS_END, 0x00020000);
  const rsrc_t ro = __builtin_amdgcn_make_buffer_rsrc((void*)wo, 0, 1024 * 1024 * 2, 0x00020000);
  const unsigned vwo = (lane >> 3) * 2048 + (((lane & 7) ^ ((((lane >> 4) & 1) << 2) | ((lane >> 5) << 1))) << 4);
  auto wo_piece = [&](int pi) { const int pos = pi >> 2, hd = (pos & 16) | ((pos + rot2) & 15); glds(ro, lds + WO_OFF + pi * 1024, vwo, (hd * 32 + (pi & 3) * 8) * 2048 + ct2 * 128); };
  const unsigned wfa = lds + WPHI_OFF + hp * 2048 + (8 * h + q) * 64 + blk * 32 + p * 8;
  const int rb = wr * 2 + (wave >> 2);
  bf16x8 wf[2];
  u32x4 kin[2], qin[2];
  if (__builtin_expect(wave >= 4, 1)) {
    const rsrc_t rx = __builtin_amdgcn_make_buffer_rsrc((void*)x, 0, 2048 * 1024 * 2, 0x00020000);
    const rsrc_t rw = __builtin_amdgcn_make_buffer_rsrc((void*)w, 0, 1024 * 3072 * 2, 0x00020000);
    const rsrc_t rp = __builtin_amdgcn_make_buffer_rsrc((void*)wphi, 0, 32 * 32 * 32 * 2, 0x00020000);
    const unsigned va = (lane >> 3) * 2048 + (((lane & 7) ^ ((4 * (cw & 1) + (lane >> 4)) & 7)) << 4);
    const unsigned vb = (lane >> 3) * 6144 + (((lane & 7) ^ ((((lane >> 4) & 1) << 2) | ((cw & 1) << 1))) << 4);
    const int cls = (ct - rt) & 3;
#define ORD0 0xfedba9c765832140ull
#define ORD1 0xfecbad8769432501ull
#define ORD2 0xfdcbe987a5436102ull
#define ORD3 0xedcfa98b65472103ull
#define ROT1 (8 * (b & 1))
    const u64 o01 = (cls & 1) ? ORD1 : ORD0, o23 = (cls & 1) ? ORD3 : ORD2, order = (cls & 2) ? o23 : o01;
    const int rot = ROT1;
    const unsigned sa0 = (rt * 128 + cw * 8) * 2048, sb0 = cw * 8 * 6144 + ct * 128;
    auto issue = [&](int t, int slot) {
      const int kt = ((int)(order >> (4 * t)) + rot) & 15;
      const unsigned dst = lds + slot * STAGE + cw * 1024, sa = sa0 + kt * 128, sb = sb0 + kt * (64 * 6144);
#pragma unroll
      for (int j = 0; j < 4; j++) glds(rx, dst + j * 4096, va, sa + j * (32 * 2048));
#pragma unroll
      for (int j = 0; j < 6; j++) glds(rw, dst + BOFF + j * 4096, vb, sb + (j >> 1) * 2048 + (j & 1) * (32 * 6144));
    };
    issue(0, 0);
    SCHED0();
    const int pa = G == 0 ? l15 : rt2, pb = G == 0 ? ct : l15;
    const int peer = G < 2 ? blk_of1(pa, pb) : blk_of2(rt, l15);
    const unsigned a_msg_in = lane < 48 ? WS_MSG + b * 512 + lane * 8 : 0xfffffff0u;
    const unsigned a_msg_out = lane < 48 ? WS_MSG + peer * 512 + (G == 0 ? rt : G == 1 ? 32 + ct2 : 16 + ct) * 8 : 0xfffffff0u;
    u32x2 m = {0u, 0u}; unsigned nonce = 1, echoed = 0;
    if (wave == 4) asm volatile("buffer_load_dwordx2 %0, %1, %2, 0 offen sc1" : "+v"(m) : "v"(a_msg_in), "s"(rs) : "memory");
    glds(rp, lds + WPHI_OFF + cw * 1024, lane * 16, ct * 4096 + cw * 1024);
    issue(1, 1);
    asm volatile("s_waitcnt vmcnt(10)\n\ts_barrier" : "+v"(m)::"memory");
    if (wave == 4) {
      while (__builtin_amdgcn_ballot_w64(m.x == nonce)) nonce++;
      if (lane == 0) __builtin_amdgcn_raw_buffer_store_b32(nonce, rs, WS_NONCE + b * 4, 0, SC1);
    }
    issue(2, 2);
    wf[0] = tr2(wfa, wfa + 1024); wf[1] = tr2(wfa + 256, wfa + 1280);
    int slot = 0;
#pragma unroll 1
    for (int t = 0; t < 13; t++) {
      asm volatile("s_waitcnt vmcnt(10) lgkmcnt(0)\n\ts_barrier" : "+v"(echoed)::"memory");
      if (wave == 4) {
        if (t == 5) asm volatile("buffer_load_dword %0, %1, %2, 0 offen sc1" : "+v"(echoed) : "v"(WS_NONCE + peer * 4), "s"(rs) : "memory");
        if (t == 6) __builtin_amdgcn_raw_buffer_store_b64((u32x2){echoed, nonce}, rs, a_msg_out, 0, SC1);
      }
      issue(t + 3, slot);
      slot = slot == NS - 1 ? 0 : slot + 1;
    }
    waitv<10>(); bar();
    if (wave == 4) asm volatile("buffer_load_dwordx2 %0, %1, %2, 0 offen sc1" : "+v"(m) : "v"(a_msg_in), "s"(rs) : "memory");
#pragma unroll
    for (int j = 0; j < 8; j++) wo_piece(88 + j * 4 + cw);
    asm volatile("s_waitcnt vmcnt(8)\n\ts_barrier" : "+v"(m)::"memory");
    if (wave == 4) {
      for (unsigned spins = 0;; spins++) {
        const bool ok = lane >= 48 || m.x == nonce;
        if (ok && lane < 48 && m.y != echoed) { echoed = m.y; __builtin_amdgcn_raw_buffer_store_b64((u32x2){echoed, nonce}, rs, a_msg_out, 0, SC1); }
        if (__builtin_amdgcn_ballot_w64(!ok) == 0 || spins > SPIN_CAP) break;
        asm volatile("buffer_load_dwordx2 %0, %1, %2, 0 offen sc1\n\ts_waitcnt vmcnt(0)" : "+v"(m) : "v"(a_msg_in), "s"(rs) : "memory");
      }
    }
#pragma unroll
    for (int j = 0; j < 8; j++) wo_piece(16 + j * 4 + cw);
    bar();
#pragma unroll
    for (int j = 0; j < 8; j++) wo_piece(48 + j * 4 + cw);
    bar();
    const unsigned hq = lds + PK_OFF + hp * 16384 + rb * 4096 + lane * 16;
    asm volatile("ds_read_b128 %0, %4\n\tds_read_b128 %1, %4 offset:1024\n\tds_read_b128 %2, %4 offset:2048\n\tds_read_b128 %3, %4 offset:3072\n\ts_waitcnt lgkmcnt(0)"
                 : "=&v"(kin[0]), "=&v"(kin[1]), "=&v"(qin[0]), "=&v"(qin[1]) : "v"(hq) : "memory");
  } else {
    {
      const u32x4 z4 = {0u, 0u, 0u, 0u};
#pragma unroll
      for (int j = 0; j < 4; j++) __builtin_amdgcn_raw_buffer_store_b128(z4, rs, lane * 16, WS_IN + b * 16384 + (cw * 4 + j) * 1024, SC1);
      if (cw == 0) __builtin_amdgcn_raw_buffer_store_b64((u32x2){0u, 0u}, rs, lane * 8, WS_Z + b * 512, SC1);
      if (cw == 1) __builtin_amdgcn_raw_buffer_store_b64((u32x2){~0u, ~0u}, rs, lane * 8, WS_KVR + ct * 8192 + rt * 512, SC1);
      if (cw == 2 && lane < 4) __builtin_amdgcn_raw_buffer_store_b64((u32x2){0u, 0u}, rs, lane * 128, WS_OFLAG + rt * 512 + ct * 8, SC1);
    }
    unsigned aa[2], bb[2];
#pragma unroll
    for (int s = 0; s < 2; s++) {
      aa[s] = lds + (wr * 64 + l15) * 128 + (((4 * s + G) ^ (l15 >> 1)) << 4);
      bb[s] = lds + BOFF + (8 * G + q) * 128 + (((hp * 4 + s * 2 + (p >> 1)) ^ ((((q >> 1) & 1) << 2) | ((G & 1) << 1))) << 4) + (p & 1) * 8;
    }
    f32x4 c[4][6] = {};
    Frag f0, f1;
    bar();
    STAMP(1)
    load_frag<0>(f0, aa[0], bb[0], bb[1]);
    wf[0] = tr2(wfa, wfa + 1024); wf[1] = tr2(wfa + 256, wfa + 1280);
    unsigned so = 0;
#pragma unroll 1
    for (int t = 0; t < 16; t++) {
      SCHED0();
      load_frag<1>(f1, aa[1] + so, bb[0] + so, bb[1] + so); mma24(c, f0); interleave(); SCHED0();
      so = so == (NS - 1) * STAGE ? 0 : so + STAGE;
      all0_bar(); SCHED0();
      load_frag<0>(f0, aa[0] + so, bb[0] + so, bb[1] + so); mma24(c, f1); interleave(); SCHED0();
    }
    STAMP(2)
    const unsigned pos = ((G & 1) << 10) + ((G >> 1) * 32 + l15) * 16;
    const unsigned hq = lds + PK_OFF + hp * 16384 + (rb + 1) * 4096 + pos, vf = lds + VF_OFF + (hp * 4 + rb) * 2048 + pos;
    st128(hq, pk44(c[2][2], c[2][3])); st128(hq + 256, pk44(c[3][2], c[3][3]));
    st128(hq + 2048, pk44(c[2][0], c[2][1])); st128(hq + 2048 + 256, pk44(c[3][0], c[3][1]));
    lgkm0_bar();
    kin[0] = pk44(c[0][2], c[0][3]); kin[1] = pk44(c[1][2], c[1][3]); swap16(kin[0], kin[1]);
    qin[0] = pk44(c[0][0], c[0][1]); qin[1] = pk44(c[1][0], c[1][1]); swap16(qin[0], qin[1]);
    st128(vf, pk44(c[0][4], c[1][4])); st128(vf + 256, pk44(c[0][5], c[1][5]));
    st128(vf + 2048, pk44(c[2][4], c[3][4])); st128(vf + 2048 + 256, pk44(c[2][5], c[3][5]));
  }
  u32x4 P[4];
  feature_map(wf, kin, P);
  { const int sg = (((r >> 1) & 1) << 3) | (r & 1) | (((r >> 2) & 3) << 1);
    const unsigned a0 = lds + PK_OFF + hp * 16384 + (rb * 32 + r) * 128 + ((sg ^ h) << 3);
#pragma unroll
    for (int j = 0; j < 8; j++) *(LDS3 u32x2*)(size_t)(a0 ^ (j << 4)) = (u32x2){P[j >> 1][2 * (j & 1)], P[j >> 1][2 * (j & 1) + 1]}; }
  lgkm0_bar();
  STAMP(3)
  {
    const int hp2 = cw >> 1, fb = cw & 1;
    unsigned ta[2];
#pragma unroll
    for (int u = 0; u < 2; u++) {
      const int s = 8 * h + 4 * u + q, sg = (((s >> 1) & 1) << 3) | (s & 1) | (((s >> 2) & 3) << 1);
      ta[u] = lds + PK_OFF + hp2 * 16384 + s * 128 + (((fb * 8 + blk * 4 + p) ^ sg) << 3);
    }
    if (wave < 4) {
      f32x16 kv = {};
#pragma unroll
      for (int ks = 0; ks < 8; ks++) kv = MFMA32(tr2(ta[ks & 1] + (ks >> 1) * 4096, ta[ks & 1] + (ks >> 1) * 4096 + 2048), ld128(lds + VF_OFF + hp2 * 8192 + ks * 1024 + lane * 16), kv);
#pragma unroll
      for (int g = 0; g < 4; g++) {
        const u32x4 v = {__float_as_uint(kv[4 * g]) | 1u, __float_as_uint(kv[4 * g + 1]) | 1u, __float_as_uint(kv[4 * g + 2]) | 1u, __float_as_uint(kv[4 * g + 3]) | 1u};
        __builtin_amdgcn_raw_buffer_store_b128(v, rs, blk_of1(cw * 4 + (g >> 1) * 2 + h, ct) * 16384 + r * 16, WS_IN + rt * 1024 + (g & 1) * 512, SC1);
      }
    } else {
      wo_piece(80 + cw); wo_piece(84 + cw); wo_piece(120 + cw); wo_piece(124 + cw);
      float z0 = 0.f, z1 = 0.f;
      const bf16x2 one = {(__bf16)1.0f, (__bf16)1.0f};
#pragma unroll
      for (int ks = 0; ks < 8; ks++) {
        const bf16x4 u0 = trd(ta[ks & 1] + (ks >> 1) * 4096), u1 = trd(ta[ks & 1] + (ks >> 1) * 4096 + 2048);
        z0 = __builtin_amdgcn_fdot2_f32_bf16(__builtin_shufflevector(u0, u0, 0, 1), one, z0, false);
        z1 = __builtin_amdgcn_fdot2_f32_bf16(__builtin_shufflevector(u0, u0, 2, 3), one, z1, false);
        z0 = __builtin_amdgcn_fdot2_f32_bf16(__builtin_shufflevector(u1, u1, 0, 1), one, z0, false);
        z1 = __builtin_amdgcn_fdot2_f32_bf16(__builtin_shufflevector(u1, u1, 2, 3), one, z1, false);
      }
      const float z = xhalf_add(z0 + z1);
      if (lane < 32) __builtin_amdgcn_raw_buffer_store_b32(__float_as_uint(z) | 1u, rs, lane * 4, WS_Z + b * 512 + cw * 128, SC1);
    }
  }
  STAMP(4)
  feature_map(wf, qin, P);
  STAMP(5)
  unsigned zpk = 0;
  if (wave < 3) {
    f32x2 sum;
    for (unsigned spins = 0;; spins++) {
      u32x2 v[16];
#pragma unroll
      for (int j = 0; j < 16; j++) v[j] = __builtin_amdgcn_raw_buffer_load_b64(rs, lane * 8, wave < 2 ? WS_IN + b * 16384 + wave * 512 + j * 1024 : WS_Z + blk_of1(j, ct) * 512, SC1);
      unsigned ok = 1u; sum = (f32x2){0.f, 0.f};
#pragma unroll
      for (int j = 0; j < 16; j++) { ok &= v[j].x & v[j].y; sum += __builtin_bit_cast(f32x2, v[j]); }
      if (__builtin_amdgcn_ballot_w64(ok == 0) == 0 || spins > SPIN_CAP) break;
      asm volatile("" ::: "memory");
    }
    zpk = pk2(sum.x, sum.y);
    if (wave < 2) __builtin_amdgcn_raw_buffer_store_b32(zpk == ~0u ? 0x7fff7fffu : zpk, rs, lane * 4, WS_KVR + ct * 8192 + rt * 512 + wave * 256, SC1);
  }
  STAMP(6)
  lgkm0_bar();
  if (wave == 2) *(LDS3 unsigned*)(size_t)(lds + 8192 + lane * 4) = zpk;
  wo_piece(wave * 2); wo_piece(wave * 2 + 1);
  { u32x4 v;
    for (unsigned spins = 0;; spins++) {
      asm volatile("buffer_load_dwordx4 %0, %1, %2, %3 offen sc1\n\ts_waitcnt vmcnt(0)" : "=&v"(v) : "v"(lane * 16), "s"(rs), "s"(WS_KVR + ct * 8192 + wave * 1024) : "memory");
      const bool bad = v.x == ~0u || v.y == ~0u || v.z == ~0u || v.w == ~0u;
      if (__builtin_amdgcn_ballot_w64(bad) == 0 || spins > SPIN_CAP) break;
    }
    st128(lds + KVF_OFF + wave * 1024 + lane * 16, v); }
  all0_bar();
  STAMP(7)
  { f32x16 num = {}, den = {};
    const unsigned ka = lds + KVF_OFF + hp * 4096 + h * 512 + r * 8, za = lds + 8192 + hp * 128 + h * 8;
#pragma unroll
    for (int j = 0; j < 4; j++) {
      num = MFMA32(cat(ld64(ka + j * 1024), ld64(ka + j * 1024 + 256)), as8(P[j]), num);
      den = MFMA32(cat(ld64(za + j * 32), ld64(za + j * 32 + 16)), as8(P[j]), den);
    }
    const float rd = __builtin_amdgcn_rcpf(den[0] + 1e-6f);
#pragma unroll
    for (int i = 0; i < 16; i++) num[i] *= rd;
    const unsigned vo = blk * 32768 + (h * 16 + l15) * 16, so = WS_O + ((rt * 8 + rb * 2) * 32 + ct * 2 + hp) * 1024;
    __builtin_amdgcn_raw_buffer_store_b128(pack8<0>(num), rs, vo, so, SC1);
    __builtin_amdgcn_raw_buffer_store_b128(pack8<1>(num), rs, vo + 512, so, SC1);
    waitv<0>();
    if (lane == 0) __builtin_amdgcn_raw_buffer_store_b32(1u, rs, 0, WS_OFLAG + rt * 512 + rb * 128 + (ct * 2 + hp) * 4, SC1);
  }
  STAMP(8)
  bar();
  const int rb2 = wave & 3, kh = wave >> 2;
  for (unsigned spins = 0;; spins++) {
    unsigned f;
    asm volatile("buffer_load_dword %0, %1, %2, %3 offen sc1\n\ts_waitcnt vmcnt(0)" : "=&v"(f) : "v"(l15 * 4), "s"(rs), "s"(WS_OFLAG + rt2 * 512 + rb2 * 128 + kh * 64) : "memory");
    if (__builtin_amdgcn_ballot_w64(f != 1u) == 0 || spins > SPIN_CAP) break;
  }
  __builtin_amdgcn_s_waitcnt(0x0f70);
  STAMP(9)
  u32x4 a[16][2];
#pragma unroll
  for (int j = 0; j < 16; j++)
#pragma unroll
    for (int m = 0; m < 2; m++) a[j][m] = __builtin_amdgcn_raw_buffer_load_b128(rs, lane * 16, WS_O + ((rt2 * 8 + rb2 * 2 + m) * 32 + kh * 16 + ((j + rot2) & 15)) * 1024, SC1);
  SCHED0();
  unsigned wb[4];
#pragma unroll
  for (int n = 0; n < 4; n++) wb[n] = lds + WO_OFF + kh * 65536 + (16 * (G >> 1) + 4 * (G & 1) + q) * 128 + (((n * 2 + (p >> 1)) ^ ((((q >> 1) & 1) << 2) | ((G & 1) << 1))) << 4) + (p & 1) * 8;
  f32x4 c[2][4] = {};
#pragma unroll
  for (int j = 0; j < 16; j++)
#pragma unroll
    for (int n = 0; n < 4; n++) {
      const bf16x8 wv = tr2(wb[n] + j * 4096, wb[n] + j * 4096 + 1024);
      c[0][n] = MFMA16(wv, as8(a[j][0]), c[0][n]);
      c[1][n] = MFMA16(wv, as8(a[j][1]), c[1][n]);
    }
  STAMP(10)
  { const unsigned wa = lds + wave * 4096 + lane * 16;
#pragma unroll
    for (int n = 0; n < 4; n++) *(LDS3 f32x4*)(size_t)(wa + n * 1024) = kh ? c[0][n] : c[1][n];
    lgkm0_bar();
    f32x4 o4[4];
#pragma unroll
    for (int n = 0; n < 4; n++) o4[n] = (kh ? c[1][n] : c[0][n]) + *(LDS3 const f32x4*)(size_t)(lds + (wave ^ 4) * 4096 + lane * 16 + n * 1024);
    __attribute__((address_space(1))) u16* dst = (__attribute__((address_space(1))) u16*)out + (size_t)(rt2 * 128 + rb2 * 32 + kh * 16 + l15) * 1024 + ct2 * 64 + (G & 1) * 16 + (G >> 1) * 8;
#pragma unroll
    for (int n = 0; n < 4; n += 2) {
      auto s0 = __builtin_amdgcn_permlane16_swap(pk2(o4[n][0], o4[n][1]), pk2(o4[n + 1][0], o4[n + 1][1]), false, false);
      auto s1 = __builtin_amdgcn_permlane16_swap(pk2(o4[n][2], o4[n][3]), pk2(o4[n + 1][2], o4[n + 1][3]), false, false);
      *(__attribute__((address_space(1))) u32x4*)(dst + n * 16) = (u32x4){s0[0], s1[0], s0[1], s1[1]};
    }
  }
  STAMP(11)
  STAMP_FLUSH
}

extern "C" void kernel_launch(void* const* d_in, const int* in_sizes, int n_in, void* d_out, int out_size, void* d_ws, size_t ws_size, hipStream_t stream) {
  (void)hipFuncSetAttribute((const void*)hedgehog_fused, hipFuncAttributeMaxDynamicSharedMemorySize, LDS_BYTES);
  hedgehog_fused<<<256, 512, LDS_BYTES, stream>>>((const u16*)d_in[0], (const u16*)d_in[1], (const u16*)d_in[2], (const u16*)d_in[3], (u16*)d_out, (char*)d_ws);
}
```

```cpp
#define OUT_AUX 16
#include <hip/hip_runtime.h>
#include <hip/hip_bf16.h>
#include <stdint.h>

typedef unsigned short u16;
typedef unsigned long long u64;
typedef __bf16 bf16x2 __attribute__((ext_vector_type(2)));
typedef __bf16 bf16x4 __attribute__((ext_vector_type(4)));
typedef __bf16 bf16x8 __attribute__((ext_vector_type(8)));
typedef float f32x2 __attribute__((ext_vector_type(2)));
typedef float f32x4 __attribute__((ext_vector_type(4)));
typedef float f32x16 __attribute__((ext_vector_type(16)));
typedef unsigned u32x2 __attribute__((ext_vector_type(2)));
typedef unsigned u32x4 __attribute__((ext_vector_type(4)));
typedef __amdgpu_buffer_rsrc_t rsrc_t;
#define LDS3 __attribute__((address_space(3)))
#define DI __device__ __forceinline__
#define MFMA32(a, b, c) __builtin_amdgcn_mfma_f32_32x32x16_bf16((a), (b), (c), 0, 0, 0)
#define MFMA16(a, b, c) __builtin_amdgcn_mfma_f32_16x16x32_bf16((a), (b), (c), 0, 0, 0)
#define SCHED0() __builtin_amdgcn_sched_barrier(0)
#define STAMP_DECL
#define STAMP(i)
#define STAMP_FLUSH
constexpr int SC1 = 16;

template <int N> DI void waitv() { asm volatile("s_waitcnt vmcnt(%0)" ::"n"(N) : "memory"); }
DI void bar() { asm volatile("s_barrier" ::: "memory"); }
DI void lgkm0_bar() { asm volatile("s_waitcnt lgkmcnt(0)\n\ts_barrier" ::: "memory"); }
DI void all0_bar() { asm volatile("s_waitcnt vmcnt(0) lgkmcnt(0)\n\ts_barrier" ::: "memory"); }
DI bf16x8 ld128(unsigned a) { return *(LDS3 const bf16x8*)(size_t)a; }
DI bf16x4 ld64(unsigned a) { return *(LDS3 const bf16x4*)(size_t)a; }
DI void st128(unsigned a, u32x4 v) { *(LDS3 u32x4*)(size_t)a = v; }
DI bf16x4 trd(unsigned a) { return __builtin_amdgcn_ds_read_tr16_b64_v4bf16((LDS3 bf16x4*)(size_t)a); }
DI bf16x8 cat(bf16x4 a, bf16x4 b) { return __builtin_shufflevector(a, b, 0, 1, 2, 3, 4, 5, 6, 7); }
DI bf16x8 tr2(unsigned a0, unsigned a1) { return cat(trd(a0), trd(a1)); }
DI unsigned pk2(float lo, float hi) { f32x2 t = {lo, hi}; return __builtin_bit_cast(unsigned, __builtin_convertvector(t, bf16x2)); }
DI u32x4 pk44(const f32x4& a, const f32x4& b) { return (u32x4){pk2(a[0], a[1]), pk2(a[2], a[3]), pk2(b[0], b[1]), pk2(b[2], b[3])}; }
template <int S> DI u32x4 pack8(const f32x16& x) {
  return (u32x4){pk2(x[8 * S], x[8 * S + 1]), pk2(x[8 * S + 2], x[8 * S + 3]), pk2(x[8 * S + 4], x[8 * S + 5]), pk2(x[8 * S + 6], x[8 * S + 7])};
}
DI bf16x8 as8(u32x4 v) { return __builtin_bit_cast(bf16x8, v); }
DI float xhalf_max(float v) { auto r = __builtin_amdgcn_permlane32_swap(__float_as_uint(v), __float_as_uint(v), false, false); return __builtin_fmaxf(__uint_as_float(r[0]), __uint_as_float(r[1])); }
DI float xhalf_min(float v) { auto r = __builtin_amdgcn_permlane32_swap(__float_as_uint(v), __float_as_uint(v), false, false); return __builtin_fminf(__uint_as_float(r[0]), __uint_as_float(r[1])); }
DI float xhalf_add(float v) { auto r = __builtin_amdgcn_permlane32_swap(__float_as_uint(v), __float_as_uint(v), false, false); return __uint_as_float(r[0]) + __uint_as_float(r[1]); }
DI void swap16(u32x4& a, u32x4& b) {
#pragma unroll
  for (int i = 0; i < 4; i++) { auto r = __builtin_amdgcn_permlane16_swap(a[i], b[i], false, false); a[i] = r[0]; b[i] = r[1]; }
}
DI void glds(rsrc_t rs, unsigned dst, unsigned voff, unsigned soff) {
  asm volatile("s_mov_b32 m0, %0\n\ts_nop 0\n\tbuffer_load_dwordx4 %1, %2, %3 offen lds" ::"s"(dst), "v"(voff), "s"(rs), "s"(soff) : "memory");
}

constexpr int NS = 3, STAGE = 40960, BOFF = 16384, WPHI_OFF = NS * STAGE;
constexpr int PK_OFF = 0, VF_OFF = 32768;
constexpr int KVF_OFF = 0, ZF_OFF = 49152 + 0;
constexpr int WO_OFF = 32768, LDS_BYTES = 163840;
constexpr unsigned WS_NONCE = 0, WS_MSG = 4096, WS_OFLAG = WS_MSG + 256 * 512, WS_Z = WS_OFLAG + 8192, WS_KVR = WS_Z + 256 * 512,
                   WS_IN = WS_KVR + 16 * 8192, WS_O = WS_IN + 256 * 16384, WS_END = WS_O + 2048 * 1024 * 2;
constexpr unsigned SPIN_CAP = 1u << 18;

DI void feature_map(const bf16x8 (&wf)[2], const u32x4 (&in)[2], u32x4 (&P)[4]) {
  f32x16 p = {};
  p = MFMA32(wf[0], as8(in[0]), p);
  p = MFMA32(wf[1], as8(in[1]), p);
  float mx = p[0], mn = p[0];
#pragma unroll
  for (int i = 1; i < 16; i++) { mx = __builtin_fmaxf(mx, p[i]); mn = __builtin_fminf(mn, p[i]); }
  mx = xhalf_max(mx); mn = xhalf_min(mn);
  const float c = 1.4426950408889634f;
  const float a = -mx * c, b = mn * c;
  f32x16 e, g; float s0 = 0.f, s1 = 0.f, t0 = 0.f, t1 = 0.f;
#pragma unroll
  for (int i = 0; i < 16; i++) { e[i] = __builtin_amdgcn_exp2f(__builtin_fmaf(p[i], c, a)); g[i] = __builtin_amdgcn_exp2f(__builtin_fmaf(p[i], -c, b)); }
#pragma unroll
  for (int i = 0; i < 16; i += 2) { s0 += e[i]; s1 += e[i + 1]; t0 += g[i]; t1 += g[i + 1]; }
  const float rs = __builtin_amdgcn_rcpf(xhalf_add(s0 + s1)), rt = __builtin_amdgcn_rcpf(xhalf_add(t0 + t1));
#pragma unroll
  for (int i = 0; i < 16; i++) { e[i] *= rs; g[i] *= rt; }
  P[0] = pack8<0>(e); P[1] = pack8<1>(e); P[2] = pack8<0>(g); P[3] = pack8<1>(g);
}

struct Frag { bf16x8 x[4], w[6]; };
template <int S> DI void load_frag(Frag& f, unsigned a, unsigned b0, unsigned b1) {
  f.x[0] = ld128(a);
#pragma unroll
  for (int g = 0; g < 3; g++) {
    f.w[2 * g] = tr2(b0 + g * 8192 + S * 4096, b0 + g * 8192 + S * 4096 + 512);
    f.w[2 * g + 1] = tr2(b1 + g * 8192 + S * 4096, b1 + g * 8192 + S * 4096 + 512);
  }
#pragma unroll
  for (int m = 1; m < 4; m++) f.x[m] = ld128(a + m * 2048);
}
DI void mma24(f32x4 (&c)[4][6], const Frag& f) {
#pragma unroll
  for (int m = 0; m < 4; m++)
#pragma unroll
    for (int n = 0; n < 6; n++) {
      if (n < 4) c[m][n] = MFMA16(f.w[n], f.x[m], c[m][n]);
      else c[m][n] = MFMA16(f.x[m], f.w[n], c[m][n]);
    }
}
DI void interleave() {
#pragma unroll
  for (int i = 0; i < 16; i++) { __builtin_amdgcn_sched_group_barrier(0x8, 1, 0); __builtin_amdgcn_sched_group_barrier(0x100, 1, 0); }
  __builtin_amdgcn_sched_group_barrier(0x8, 8, 0);
}

DI int blk_of1(int rt, int ct) { return ((rt & 7) + 8 * (ct & 3)) * 8 + (rt >> 3) + 2 * (ct >> 2); }
DI int blk_of2(int rt, int ct) { return ((rt & 3) + 4 * (ct & 7)) * 8 + (rt >> 2) + 4 * (ct >> 3); }

__global__ __launch_bounds__(512, 2) void hedgehog_fused(const u16* __restrict__ x, const u16* __restrict__ w, const u16* __restrict__ wphi,
                                                         const u16* __restrict__ wo, u16* __restrict__ out, char* __restrict__ ws) {
  extern __shared__ __attribute__((aligned(16))) char smem[];
  STAMP_DECL
  STAMP(0)
  const unsigned lds = (unsigned)(size_t)(LDS3 char*)smem;
  const int b = blockIdx.x, xcd = b & 7, bi = b >> 3;
  const int rt = (xcd & 1) * 8 + (bi & 7), ct = (xcd >> 1) * 4 + (bi >> 3);
  const int rt2 = (xcd & 3) * 4 + (bi & 3), ct2 = (xcd >> 2) * 8 + (bi >> 2);
  const int rot2 = (4 * (bi & 3) + 2 * (bi >> 2)) & 15;
  const int wave = __builtin_amdgcn_readfirstlane(threadIdx.x >> 6), lane = threadIdx.x & 63;
  const int cw = wave & 3, wr = cw >> 1, hp = cw & 1;
  const int r = lane & 31, h = lane >> 5, q = (lane & 15) >> 2, p = lane & 3, blk = (lane >> 4) & 1, G = lane >> 4, l15 = lane & 15;
  asm volatile("" : "+s"(x), "+s"(w), "+s"(wphi), "+s"(wo), "+s"(out), "+s"(ws));
  const rsrc_t rs = __builtin_amdgcn_make_buffer_rsrc(ws, 0, WS_END, 0x00020000);
  const rsrc_t ro = __builtin_amdgcn_make_buffer_rsrc((void*)wo, 0, 1024 * 1024 * 2, 0x00020000);
  const unsigned vwo = (lane >> 3) * 2048 + (((lane & 7) ^ ((((lane >> 4) & 1) << 2) | ((lane >> 5) << 1))) << 4);
  auto wo_piece = [&](int pi) { const int pos = pi >> 2, hd = (pos & 16) | ((pos + rot2) & 15); glds(ro, lds + WO_OFF + pi * 1024, vwo, (hd * 32 + (pi & 3) * 8) * 2048 + ct2 * 128); };
  const unsigned wfa = lds + WPHI_OFF + hp * 2048 + (8 * h + q) * 64 + blk * 32 + p * 8;
  const int rb = wr * 2 + (wave >> 2);
  bf16x8 wf[2];
  u32x4 kin[2], qin[2];
  if (__builtin_expect(wave >= 4, 1)) {
    const rsrc_t rx = __builtin_amdgcn_make_buffer_rsrc((void*)x, 0, 2048 * 1024 * 2, 0x00020000);
    const rsrc_t rw = __builtin_amdgcn_make_buffer_rsrc((void*)w, 0, 1024 * 3072 * 2, 0x00020000);
    const rsrc_t rp = __builtin_amdgcn_make_buffer_rsrc((void*)wphi, 0, 32 * 32 * 32 * 2, 0x00020000);
    const unsigned va = (lane >> 3) * 2048 + (((lane & 7) ^ ((4 * (cw & 1) + (lane >> 4)) & 7)) << 4);
    const unsigned vb = (lane >> 3) * 6144 + (((lane & 7) ^ ((((lane >> 4) & 1) << 2) | ((cw & 1) << 1))) << 4);
    const int cls = (ct - rt) & 3;
#define ORD0 0xfedba9c765832140ull
#define ORD1 0xfecbad8769432501ull
#define ORD2 0xfdcbe987a5436102ull
#define ORD3 0xedcfa98b65472103ull
#define ROT1 (8 * (b & 1))
    const u64 o01 = (cls & 1) ? ORD1 : ORD0, o23 = (cls & 1) ? ORD3 : ORD2, order = (cls & 2) ? o23 : o01;
    const int rot = ROT1;
    const unsigned sa0 = (rt * 128 + cw * 8) * 2048, sb0 = cw * 8 * 6144 + ct * 128;
    auto issue = [&](int t, int slot) {
      const int kt = ((int)(order >> (4 * t)) + rot) & 15;
      const unsigned dst = lds + slot * STAGE + cw * 1024, sa = sa0 + kt * 128, sb = sb0 + kt * (64 * 6144);
#pragma unroll
      for (int j = 0; j < 4; j++) glds(rx, dst + j * 4096, va, sa + j * (32 * 2048));
#pragma unroll
      for (int j = 0; j < 6; j++) glds(rw, dst + BOFF + j * 4096, vb, sb + (j >> 1) * 2048 + (j & 1) * (32 * 6144));
    };
    issue(0, 0);
    SCHED0();
    const int pa = G == 0 ? l15 : rt2, pb = G == 0 ? ct : l15;
    const int peer = G < 2 ? blk_of1(pa, pb) : blk_of2(rt, l15);
    const unsigned a_msg_in = lane < 48 ? WS_MSG + b * 512 + lane * 8 : 0xfffffff0u;
    const unsigned a_msg_out = lane < 48 ? WS_MSG + peer * 512 + (G == 0 ? rt : G == 1 ? 32 + ct2 : 16 + ct) * 8 : 0xfffffff0u;
    u32x2 m = {0u, 0u}; unsigned nonce = 1, echoed = 0;
    if (wave == 4) asm volatile("buffer_load_dwordx2 %0, %1, %2, 0 offen sc1" : "+v"(m) : "v"(a_msg_in), "s"(rs) : "memory");
    glds(rp, lds + WPHI_OFF + cw * 1024, lane * 16, ct * 4096 + cw * 1024);
    issue(1, 1);
    asm volatile("s_waitcnt vmcnt(10)\n\ts_barrier" : "+v"(m)::"memory");
    if (wave == 4) {
      while (__builtin_amdgcn_ballot_w64(m.x == nonce)) nonce++;
      if (lane == 0) __builtin_amdgcn_raw_buffer_store_b32(nonce, rs, WS_NONCE + b * 4, 0, SC1);
    }
    issue(2, 2);
    wf[0] = tr2(wfa, wfa + 1024); wf[1] = tr2(wfa + 256, wfa + 1280);
    int slot = 0;
#pragma unroll 1
    for (int t = 0; t < 13; t++) {
      asm volatile("s_waitcnt vmcnt(10) lgkmcnt(0)\n\ts_barrier" : "+v"(echoed)::"memory");
      if (wave == 4) {
        if (t == 5) asm volatile("buffer_load_dword %0, %1, %2, 0 offen sc1" : "+v"(echoed) : "v"(WS_NONCE + peer * 4), "s"(rs) : "memory");
        if (t == 6) __builtin_amdgcn_raw_buffer_store_b64((u32x2){echoed, nonce}, rs, a_msg_out, 0, SC1);
      }
      issue(t + 3, slot);
      slot = slot == NS - 1 ? 0 : slot + 1;
    }
    waitv<10>(); bar();
    if (wave == 4) asm volatile("buffer_load_dwordx2 %0, %1, %2, 0 offen sc1" : "+v"(m) : "v"(a_msg_in), "s"(rs) : "memory");
#pragma unroll
    for (int j = 0; j < 8; j++) wo_piece(88 + j * 4 + cw);
    asm volatile("s_waitcnt vmcnt(8)\n\ts_barrier" : "+v"(m)::"memory");
    if (wave == 4) {
      for (unsigned spins = 0;; spins++) {
        const bool ok = lane >= 48 || m.x == nonce;
        if (ok && lane < 48 && m.y != echoed) { echoed = m.y; __builtin_amdgcn_raw_buffer_store_b64((u32x2){echoed, nonce}, rs, a_msg_out, 0, SC1); }
        if (__builtin_amdgcn_ballot_w64(!ok) == 0 || spins > SPIN_CAP) break;
        asm volatile("buffer_load_dwordx2 %0, %1, %2, 0 offen sc1\n\ts_waitcnt vmcnt(0)" : "+v"(m) : "v"(a_msg_in), "s"(rs) : "memory");
      }
    }
#pragma unroll
    for (int j = 0; j < 8; j++) wo_piece(16 + j * 4 + cw);
    bar();
#pragma unroll
    for (int j = 0; j < 8; j++) wo_piece(48 + j * 4 + cw);
    bar();
    const unsigned hq = lds + PK_OFF + hp * 16384 + rb * 4096 + lane * 16;
    asm volatile("ds_read_b128 %0, %4\n\tds_read_b128 %1, %4 offset:1024\n\tds_read_b128 %2, %4 offset:2048\n\tds_read_b128 %3, %4 offset:3072\n\ts_waitcnt lgkmcnt(0)"
                 : "=&v"(kin[0]), "=&v"(kin[1]), "=&v"(qin[0]), "=&v"(qin[1]) : "v"(hq) : "memory");
  } else {
    {
      const u32x4 z4 = {0u, 0u, 0u, 0u};
#pragma unroll
      for (int j = 0; j < 4; j++) __builtin_amdgcn_raw_buffer_store_b128(z4, rs, lane * 16, WS_IN + b * 16384 + (cw * 4 + j) * 1024, SC1);
      if (cw == 0) __builtin_amdgcn_raw_buffer_store_b64((u32x2){0u, 0u}, rs, lane * 8, WS_Z + b * 512, SC1);
      if (cw == 1) __builtin_amdgcn_raw_buffer_store_b64((u32x2){~0u, ~0u}, rs, lane * 8, WS_KVR + ct * 8192 + rt * 512, SC1);
      if (cw == 2 && lane < 4) __builtin_amdgcn_raw_buffer_store_b64((u32x2){0u, 0u}, rs, lane * 128, WS_OFLAG + rt * 512 + ct * 8, SC1);
    }
    unsigned aa[2], bb[2];
#pragma unroll
    for (int s = 0; s < 2; s++) {
      aa[s] = lds + (wr * 64 + l15) * 128 + (((4 * s + G) ^ (l15 >> 1)) << 4);
      bb[s] = lds + BOFF + (8 * G + q) * 128 + (((hp * 4 + s * 2 + (p >> 1)) ^ ((((q >> 1) & 1) << 2) | ((G & 1) << 1))) << 4) + (p & 1) * 8;
    }
    f32x4 c[4][6] = {};
    Frag f0, f1;
    bar();
    STAMP(1)
    load_frag<0>(f0, aa[0], bb[0], bb[1]);
    wf[0] = tr2(wfa, wfa + 1024); wf[1] = tr2(wfa + 256, wfa + 1280);
    unsigned so = 0;
#pragma unroll 1
    for (int t = 0; t < 16; t++) {
      SCHED0();
      load_frag<1>(f1, aa[1] + so, bb[0] + so, bb[1] + so); mma24(c, f0); interleave(); SCHED0();
      so = so == (NS - 1) * STAGE ? 0 : so + STAGE;
      all0_bar(); SCHED0();
      load_frag<0>(f0, aa[0] + so, bb[0] + so, bb[1] + so); mma24(c, f1); interleave(); SCHED0();
    }
    STAMP(2)
    const unsigned pos = ((G & 1) << 10) + ((G >> 1) * 32 + l15) * 16;
    const unsigned hq = lds + PK_OFF + hp * 16384 + (rb + 1) * 4096 + pos, vf = lds + VF_OFF + (hp * 4 + rb) * 2048 + pos;
    st128(hq, pk44(c[2][2], c[2][3])); st128(hq + 256, pk44(c[3][2], c[3][3]));
    st128(hq + 2048, pk44(c[2][0], c[2][1])); st128(hq + 2048 + 256, pk44(c[3][0], c[3][1]));
    lgkm0_bar();
    kin[0] = pk44(c[0][2], c[0][3]); kin[1] = pk44(c[1][2], c[1][3]); swap16(kin[0], kin[1]);
    qin[0] = pk44(c[0][0], c[0][1]); qin[1] = pk44(c[1][0], c[1][1]); swap16(qin[0], qin[1]);
    st128(vf, pk44(c[0][4], c[1][4])); st128(vf + 256, pk44(c[0][5], c[1][5]));
    st128(vf + 2048, pk44(c[2][4], c[3][4])); st128(vf + 2048 + 256, pk44(c[2][5], c[3][5]));
  }
  u32x4 P[4];
  feature_map(wf, kin, P);
  { const int sg = (((r >> 1) & 1) << 3) | (r & 1) | (((r >> 2) & 3) << 1);
    const unsigned a0 = lds + PK_OFF + hp * 16384 + (rb * 32 + r) * 128 + ((sg ^ h) << 3);
#pragma unroll
    for (int j = 0; j < 8; j++) *(LDS3 u32x2*)(size_t)(a0 ^ (j << 4)) = (u32x2){P[j >> 1][2 * (j & 1)], P[j >> 1][2 * (j & 1) + 1]}; }
  lgkm0_bar();
  STAMP(3)
  {
    const int hp2 = cw >> 1, fb = cw & 1;
    unsigned ta[2];
#pragma unroll
    for (int u = 0; u < 2; u++) {
      const int s = 8 * h + 4 * u + q, sg = (((s >> 1) & 1) << 3) | (s & 1) | (((s >> 2) & 3) << 1);
      ta[u] = lds + PK_OFF + hp2 * 16384 + s * 128 + (((fb * 8 + blk * 4 + p) ^ sg) << 3);
    }
    if (wave < 4) {
      f32x16 kv = {};
#pragma unroll
      for (int ks = 0; ks < 8; ks++) kv = MFMA32(tr2(ta[ks & 1] + (ks >> 1) * 4096, ta[ks & 1] + (ks >> 1) * 4096 + 2048), ld128(lds + VF_OFF + hp2 * 8192 + ks * 1024 + lane * 16), kv);
#pragma unroll
      for (int g = 0; g < 4; g++) {
        const u32x4 v = {__float_as_uint(kv[4 * g]) | 1u, __float_as_uint(kv[4 * g + 1]) | 1u, __float_as_uint(kv[4 * g + 2]) | 1u, __float_as_uint(kv[4 * g + 3]) | 1u};
        __builtin_amdgcn_raw_buffer_store_b128(v, rs, blk_of1(cw * 4 + (g >> 1) * 2 + h, ct) * 16384 + r * 16, WS_IN + rt * 1024 + (g & 1) * 512, SC1);
      }
    } else {
      wo_piece(80 + cw); wo_piece(84 + cw); wo_piece(120 + cw); wo_piece(124 + cw);
      float z0 = 0.f, z1 = 0.f;
      const bf16x2 one = {(__bf16)1.0f, (__bf16)1.0f};
#pragma unroll
      for (int ks = 0; ks < 8; ks++) {
        const bf16x4 u0 = trd(ta[ks & 1] + (ks >> 1) * 4096), u1 = trd(ta[ks & 1] + (ks >> 1) * 4096 + 2048);
        z0 = __builtin_amdgcn_fdot2_f32_bf16(__builtin_shufflevector(u0, u0, 0, 1), one, z0, false);
        z1 = __builtin_amdgcn_fdot2_f32_bf16(__builtin_shufflevector(u0, u0, 2, 3), one, z1, false);
        z0 = __builtin_amdgcn_fdot2_f32_bf16(__builtin_shufflevector(u1, u1, 0, 1), one, z0, false);
        z1 = __builtin_amdgcn_fdot2_f32_bf16(__builtin_shufflevector(u1, u1, 2, 3), one, z1, false);
      }
      const float z = xhalf_add(z0 + z1);
      if (lane < 32) __builtin_amdgcn_raw_buffer_store_b32(__float_as_uint(z) | 1u, rs, lane * 4, WS_Z + b * 512 + cw * 128, SC1);
    }
  }
  STAMP(4)
  feature_map(wf, qin, P);
  STAMP(5)
  unsigned zpk = 0;
  if (wave < 3) {
    f32x2 sum;
    for (unsigned spins = 0;; spins++) {
      u32x2 v[16];
#pragma unroll
      for (int j = 0; j < 16; j++) v[j] = __builtin_amdgcn_raw_buffer_load_b64(rs, lane * 8, wave < 2 ? WS_IN + b * 16384 + wave * 512 + j * 1024 : WS_Z + blk_of1(j, ct) * 512, SC1);
      unsigned ok = 1u; sum = (f32x2){0.f, 0.f};
#pragma unroll
      for (int j = 0; j < 16; j++) { ok &= v[j].x & v[j].y; sum += __builtin_bit_cast(f32x2, v[j]); }
      if (__builtin_amdgcn_ballot_w64(ok == 0) == 0 || spins > SPIN_CAP) break;
      asm volatile("" ::: "memory");
    }
    zpk = pk2(sum.x, sum.y);
    if (wave < 2) __builtin_amdgcn_raw_buffer_store_b32(zpk == ~0u ? 0x7fff7fffu : zpk, rs, lane * 4, WS_KVR + ct * 8192 + rt * 512 + wave * 256, SC1);
  }
  STAMP(6)
  lgkm0_bar();
  if (wave == 2) *(LDS3 unsigned*)(size_t)(lds + 8192 + lane * 4) = zpk;
  wo_piece(wave * 2); wo_piece(wave * 2 + 1);
  { u32x4 v;
    for (unsigned spins = 0;; spins++) {
      asm volatile("buffer_load_dwordx4 %0, %1, %2, %3 offen sc1\n\ts_waitcnt vmcnt(0)" : "=&v"(v) : "v"(lane * 16), "s"(rs), "s"(WS_KVR + ct * 8192 + wave * 1024) : "memory");
      const bool bad = v.x == ~0u || v.y == ~0u || v.z == ~0u || v.w == ~0u;
      if (__builtin_amdgcn_ballot_w64(bad) == 0 || spins > SPIN_CAP) break;
    }
    st128(lds + KVF_OFF + wave * 1024 + lane * 16, v); }
  all0_bar();
  STAMP(7)
  { f32x16 num = {}, den = {};
    const unsigned ka = lds + KVF_OFF + hp * 4096 + h * 512 + r * 8, za = lds + 8192 + hp * 128 + h * 8;
#pragma unroll
    for (int j = 0; j < 4; j++) {
      num = MFMA32(cat(ld64(ka + j * 1024), ld64(ka + j * 1024 + 256)), as8(P[j]), num);
      den = MFMA32(cat(ld64(za + j * 32), ld64(za + j * 32 + 16)), as8(P[j]), den);
    }
    const float rd = __builtin_amdgcn_rcpf(den[0] + 1e-6f);
#pragma unroll
    for (int i = 0; i < 16; i++) num[i] *= rd;
    const unsigned vo = blk * 32768 + (h * 16 + l15) * 16, so = WS_O + ((rt * 8 + rb * 2) * 32 + ct * 2 + hp) * 1024;
    __builtin_amdgcn_raw_buffer_store_b128(pack8<0>(num), rs, vo, so, SC1);
    __builtin_amdgcn_raw_buffer_store_b128(pack8<1>(num), rs, vo + 512, so, SC1);
    waitv<0>();
    if (lane == 0) __builtin_amdgcn_raw_buffer_store_b32(1u, rs, 0, WS_OFLAG + rt * 512 + rb * 128 + (ct * 2 + hp) * 4, SC1);
  }
  STAMP(8)
  bar();
  const int rb2 = wave & 3, kh = wave >> 2;
  for (unsigned spins = 0;; spins++) {
    unsigned f;
    asm volatile("buffer_load_dword %0, %1, %2, %3 offen sc1\n\ts_waitcnt vmcnt(0)" : "=&v"(f) : "v"(l15 * 4), "s"(rs), "s"(WS_OFLAG + rt2 * 512 + rb2 * 128 + kh * 64) : "memory");
    if (__builtin_amdgcn_ballot_w64(f != 1u) == 0 || spins > SPIN_CAP) break;
  }
  __builtin_amdgcn_s_waitcnt(0x0f70);
  STAMP(9)
  u32x4 a[16][2];
#pragma unroll
  for (int j = 0; j < 16; j++)
#pragma unroll
    for (int m = 0; m < 2; m++) a[j][m] = __builtin_amdgcn_raw_buffer_load_b128(rs, lane * 16, WS_O + ((rt2 * 8 + rb2 * 2 + m) * 32 + kh * 16 + ((j + rot2) & 15)) * 1024, SC1);
  SCHED0();
  unsigned wb[4];
#pragma unroll
  for (int n = 0; n < 4; n++) wb[n] = lds + WO_OFF + kh * 65536 + (16 * (G >> 1) + 4 * (G & 1) + q) * 128 + (((n * 2 + (p >> 1)) ^ ((((q >> 1) & 1) << 2) | ((G & 1) << 1))) << 4) + (p & 1) * 8;
  f32x4 c[2][4] = {};
#pragma unroll
  for (int j = 0; j < 16; j++)
#pragma unroll
    for (int n = 0; n < 4; n++) {
      const bf16x8 wv = tr2(wb[n] + j * 4096, wb[n] + j * 4096 + 1024);
      c[0][n] = MFMA16(wv, as8(a[j][0]), c[0][n]);
      c[1][n] = MFMA16(wv, as8(a[j][1]), c[1][n]);
    }
  STAMP(10)
  { const unsigned wa = lds + wave * 4096 + lane * 16;
#pragma unroll
    for (int n = 0; n < 4; n++) *(LDS3 f32x4*)(size_t)(wa + n * 1024) = kh ? c[0][n] : c[1][n];
    lgkm0_bar();
    f32x4 o4[4];
#pragma unroll
    for (int n = 0; n < 4; n++) o4[n] = (kh ? c[1][n] : c[0][n]) + *(LDS3 const f32x4*)(size_t)(lds + (wave ^ 4) * 4096 + lane * 16 + n * 1024);
    const rsrc_t rout = __builtin_amdgcn_make_buffer_rsrc(out, 0, 2048 * 1024 * 2, 0x00020000);
    const unsigned vo = l15 * 2048 + (G & 1) * 32 + (G >> 1) * 16, so = (rt2 * 128 + rb2 * 32 + kh * 16) * 2048 + ct2 * 128;
#pragma unroll
    for (int n = 0; n < 4; n += 2) {
      auto s0 = __builtin_amdgcn_permlane16_swap(pk2(o4[n][0], o4[n][1]), pk2(o4[n + 1][0], o4[n + 1][1]), false, false);
      auto s1 = __builtin_amdgcn_permlane16_swap(pk2(o4[n][2], o4[n][3]), pk2(o4[n + 1][2], o4[n + 1][3]), false, false);
      __builtin_amdgcn_raw_buffer_store_b128((u32x4){s0[0], s1[0], s0[1], s1[1]}, rout, vo + n * 32, so, OUT_AUX);
    }
  }
  STAMP(11)
  STAMP_FLUSH
}

extern "C" void kernel_launch(void* const* d_in, const int* in_sizes, int n_in, void* d_out, int out_size, void* d_ws, size_t ws_size, hipStream_t stream) {
  (void)hipFuncSetAttribute((const void*)hedgehog_fused, hipFuncAttributeMaxDynamicSharedMemorySize, LDS_BYTES);
  hedgehog_fused<<<256, 512, LDS_BYTES, stream>>>((const u16*)d_in[0], (const u16*)d_in[1], (const u16*)d_in[2], (const u16*)d_in[3], (u16*)d_out, (char*)d_ws);
}
```

```cpp
#include <hip/hip_runtime.h>
#include <hip/hip_bf16.h>
#include <stdint.h>

typedef unsigned short u16;
typedef unsigned long long u64;
typedef __bf16 bf16x2 __attribute__((ext_vector_type(2)));
typedef __bf16 bf16x4 __attribute__((ext_vector_type(4)));
typedef __bf16 bf16x8 __attribute__((ext_vector_type(8)));
typedef float f32x2 __attribute__((ext_vector_type(2)));
typedef float f32x4 __attribute__((ext_vector_type(4)));
typedef float f32x16 __attribute__((ext_vector_type(16)));
typedef unsigned u32x2 __attribute__((ext_vector_type(2)));
typedef unsigned u32x4 __attribute__((ext_vector_type(4)));
typedef __amdgpu_buffer_rsrc_t rsrc_t;
#define LDS3 __attribute__((address_space(3)))
#define DI __device__ __forceinline__
#define MFMA32(a, b, c) __builtin_amdgcn_mfma_f32_32x32x16_bf16((a), (b), (c), 0, 0, 0)
#define MFMA16(a, b, c) __builtin_amdgcn_mfma_f32_16x16x32_bf16((a), (b), (c), 0, 0, 0)
#define SCHED0() __builtin_amdgcn_sched_barrier(0)
constexpr int WO1 = 4, WO2 = 10, WO3 = 20, WO4 = 26, WOSLP = 12, PACE1 = 5, PACE2 = 4, SPF0 = 3, BAH = 6, AHEAD = 2, FGJ = 2;
constexpr u64 KRAMPS = 0;
constexpr u64 KRAMP = 0x12300;
constexpr int PROT = 6, NSPEC = 6;
static_assert(WO1 <= 16 && WO3 <= 26 && WO4 >= 26 && BAH <= 16 - PROT && FGJ + AHEAD <= NSPEC, "LDS reuse order");
constexpr int SC1 = 16;

template <int N> struct Int { static constexpr int value = N; };
template <int N> DI void waitv() { asm volatile("s_waitcnt vmcnt(%0)" ::"n"(N) : "memory"); }
DI void bar() { asm volatile("s_barrier" ::: "memory"); }
DI void lgkm0_bar() { asm volatile("s_waitcnt lgkmcnt(0)\n\ts_barrier" ::: "memory"); }
DI bf16x8 ld128(unsigned a) { return *(LDS3 const bf16x8*)(size_t)a; }
DI bf16x4 trd(unsigned a) { return __builtin_amdgcn_ds_read_tr16_b64_v4bf16((LDS3 bf16x4*)(size_t)a); }
DI bf16x8 cat(bf16x4 a, bf16x4 b) { return __builtin_shufflevector(a, b, 0, 1, 2, 3, 4, 5, 6, 7); }
DI bf16x8 tr2(unsigned a0, unsigned a1) { return cat(trd(a0), trd(a1)); }
DI unsigned pk2(float lo, float hi) { f32x2 t = {lo, hi}; return __builtin_bit_cast(unsigned, __builtin_convertvector(t, bf16x2)); }
template <int S> DI u32x4 pack8(const f32x16& x) {
  return (u32x4){pk2(x[8 * S], x[8 * S + 1]), pk2(x[8 * S + 2], x[8 * S + 3]), pk2(x[8 * S + 4], x[8 * S + 5]), pk2(x[8 * S + 6], x[8 * S + 7])};
}
DI bf16x8 as8(u32x4 v) { return __builtin_bit_cast(bf16x8, v); }
DI float xhalf_add(float v) { auto r = __builtin_amdgcn_permlane32_swap(__float_as_uint(v), __float_as_uint(v), false, false); return __uint_as_float(r[0]) + __uint_as_float(r[1]); }
DI unsigned umax(unsigned a, unsigned b) { return a > b ? a : b; }

DI void glds(rsrc_t rs, unsigned dst, unsigned voff, unsigned soff) {
  asm volatile("s_mov_b32 m0, %0\n\ts_nop 0\n\tbuffer_load_dwordx4 %1, %2, %3 offen lds" ::"s"(dst), "v"(voff), "s"(rs), "s"(soff) : "memory");
}
template <int DL, int DG> DI void glds2(rsrc_t rs, unsigned dst, unsigned voff, unsigned soff) {
  unsigned t;
  asm volatile("s_mov_b32 m0, %1\n\ts_add_u32 %0, %4, %6\n\tbuffer_load_dwordx4 %2, %3, %4 offen lds\n\ts_add_u32 m0, %1, %5\n\ts_nop 0\n\tbuffer_load_dwordx4 %2, %3, %0 offen lds"
               : "=&s"(t) : "s"(dst), "v"(voff), "s"(rs), "s"(soff), "n"(DL), "n"(DG) : "memory", "scc");
}
DI void ld2_async(rsrc_t rs, unsigned voff) { asm volatile("buffer_load_dwordx2 v[254:255], %0, %1, 0 offen sc1" ::"v"(voff), "s"(rs) : "memory", "v254", "v255"); }
DI u32x2 ld2_get() { u32x2 r; asm volatile("v_mov_b32 %0, v254\n\tv_mov_b32 %1, v255" : "=v"(r.x), "=v"(r.y)::"memory"); return r; }
template <int OFF = 0> DI void st16(u32x4 v, rsrc_t rs, unsigned voff, unsigned soff) {
  asm volatile("buffer_store_dwordx4 %0, %1, %2, %3 offen offset:%4 sc1\n\ts_nop 1" ::"v"(v), "v"(voff), "s"(rs), "s"(soff), "n"(OFF) : "memory");
}
template <int OFF> DI void spf(rsrc_t rs, unsigned soff) { asm volatile("s_buffer_load_dword s100, %0, %1 offset:%2" ::"s"(rs), "s"(soff), "n"(OFF) : "s100"); }
constexpr unsigned OOB = 0x80000000u;
constexpr int WPHI_OFF = 0, ZF_OFF = 4096, ZFLAG_OFF = 5120;
constexpr int ST_OFF = 8192, NS = 3, STAGE = 40960, BOFF = 16384;
constexpr int VF_OFF = 8192, PK_OFF = 24576, KVF_OFF = VF_OFF;
constexpr int WO_OFF = 32768, LDS_BYTES = 163840;
constexpr unsigned WS_NONCE = 0, WS_MSG = 8192, WS_OFLAG = WS_MSG + 256 * 512, WS_HINT = WS_OFLAG + 16 * 64 * 128, WS_Z = WS_HINT + 256 * 128, WS_KVR = WS_Z + 256 * 512,
                   WS_IN = WS_KVR + 16 * 8192, WS_O = WS_IN + 256 * 8192, WS_END = WS_O + 2048 * 1024 * 2;
constexpr u64 ORD0 = 0xfb3eda9c86574210ull, ORD1 = 0xebf3cad976852401ull, ORD2 = 0xfebdc3a879642510ull, ORD3 = 0xfe3dbc98a7542601ull;
constexpr unsigned SPIN_CAP = 1u << 18;

DI float max3(float a, float b, float c) { return __builtin_fmaxf(__builtin_fmaxf(a, b), c); }
DI float min3(float a, float b, float c) { return __builtin_fminf(__builtin_fminf(a, b), c); }
template <class E> DI void dual_softmax2(const f32x16 (&p)[2], E emit) {
  const float c = 1.4426950408889634f;
  const f32x2 c2 = {c, c};
  float mx[2], nx[2];
  unsigned sign = 0x80000000u;
  asm("" : "+s"(sign));
  const float low = -3.4028234663852886e38f;
#pragma unroll
  for (int i = 0; i < 16; i += 2)
#pragma unroll
    for (int k = 0; k < 2; k++) { mx[k] = max3(p[k][i], p[k][i + 1], i ? mx[k] : low); nx[k] = min3(p[k][i], p[k][i + 1], i ? nx[k] : -low); }
  f32x2 a2[2], b2[2];
#pragma unroll
  for (int k = 0; k < 2; k++) {
    auto m2 = __builtin_amdgcn_permlane32_swap(__float_as_uint(mx[k]), __float_as_uint(nx[k]) ^ sign, false, false);
    const float off = max3(__uint_as_float(m2[0]), __uint_as_float(m2[1]), low) * -c;
    auto o2 = __builtin_amdgcn_permlane32_swap(__float_as_uint(off), __float_as_uint(off), false, false);
    a2[k] = (f32x2){__uint_as_float(o2[0]), __uint_as_float(o2[0])}; b2[k] = (f32x2){__uint_as_float(o2[1]), __uint_as_float(o2[1])};
  }
  SCHED0();
  f32x2 e[2][8], g[2][8], s[2], t[2];
#pragma unroll
  for (int i = 0; i < 8; i++)
#pragma unroll
    for (int k = 0; k < 2; k++) {
      const f32x2 v = {p[k][2 * i], p[k][2 * i + 1]}, u = __builtin_elementwise_fma(v, c2, a2[k]), w = __builtin_elementwise_fma(v, -c2, b2[k]);
      e[k][i] = (f32x2){__builtin_amdgcn_exp2f(u.x), __builtin_amdgcn_exp2f(u.y)};
      g[k][i] = (f32x2){__builtin_amdgcn_exp2f(w.x), __builtin_amdgcn_exp2f(w.y)};
      if (i == 0) { s[k] = e[k][0]; t[k] = g[k][0]; } else { s[k] += e[k][i]; t[k] += g[k][i]; }
    }
  SCHED0();
  f32x2 rs[2], rt[2];
#pragma unroll
  for (int k = 0; k < 2; k++) {
    auto r = __builtin_amdgcn_permlane32_swap(__float_as_uint(s[k].x + s[k].y), __float_as_uint(t[k].x + t[k].y), false, false);
    const float inv = __builtin_amdgcn_rcpf(__uint_as_float(r[0]) + __uint_as_float(r[1]));
    auto ri = __builtin_amdgcn_permlane32_swap(__float_as_uint(inv), __float_as_uint(inv), false, false);
    rs[k] = (f32x2){__uint_as_float(ri[0]), __uint_as_float(ri[0])}; rt[k] = (f32x2){__uint_as_float(ri[1]), __uint_as_float(ri[1])};
  }
  SCHED0();
#pragma unroll
  for (int j = 0; j < 4; j++)
#pragma unroll
    for (int k = 0; k < 2; k++) {
      const f32x2 a0 = e[k][2 * j] * rs[k], a1 = e[k][2 * j + 1] * rs[k], b0 = g[k][2 * j] * rt[k], b1 = g[k][2 * j + 1] * rt[k];
      emit(k, j, (u32x2){pk2(a0.x, a0.y), pk2(a1.x, a1.y)});
      emit(k, 4 + j, (u32x2){pk2(b0.x, b0.y), pk2(b1.x, b1.y)});
    }
}
DI f32x16 project(const bf16x8 (&wf)[2], u32x4 i0, u32x4 i1) { f32x16 p = {}; p = MFMA32(wf[0], as8(i0), p); return MFMA32(wf[1], as8(i1), p); }

#define G1_LOOP \
  "v_mov_b64 v[128:129], 0\n\t" \
  "v_mov_b64 v[130:131], 0\n\t" \
  "v_mov_b64 v[132:133], 0\n\t" \
  "v_mov_b64 v[134:135], 0\n\t" \
  "v_mov_b64 v[136:137], 0\n\t" \
  "v_mov_b64 v[138:139], 0\n\t" \
  "v_mov_b64 v[140:141], 0\n\t" \
  "v_mov_b64 v[142:143], 0\n\t" \
  "v_mov_b64 v[144:145], 0\n\t" \
  "v_mov_b64 v[146:147], 0\n\t" \
  "v_mov_b64 v[148:149], 0\n\t" \
  "v_mov_b64 v[150:151], 0\n\t" \
  "v_mov_b64 v[152:153], 0\n\t" \
  "v_mov_b64 v[154:155], 0\n\t" \
  "v_mov_b64 v[156:157], 0\n\t" \
  "v_mov_b64 v[158:159], 0\n\t" \
  "v_mov_b64 v[160:161], 0\n\t" \
  "v_mov_b64 v[162:163], 0\n\t" \
  "v_mov_b64 v[164:165], 0\n\t" \
  "v_mov_b64 v[166:167], 0\n\t" \
  "v_mov_b64 v[168:169], 0\n\t" \
  "v_mov_b64 v[170:171], 0\n\t" \
  "v_mov_b64 v[172:173], 0\n\t" \
  "v_mov_b64 v[174:175], 0\n\t" \
  "v_mov_b64 v[176:177], 0\n\t" \
  "v_mov_b64 v[178:179], 0\n\t" \
  "v_mov_b64 v[180:181], 0\n\t" \
  "v_mov_b64 v[182:183], 0\n\t" \
  "v_mov_b64 v[184:185], 0\n\t" \
  "v_mov_b64 v[186:187], 0\n\t" \
  "v_mov_b64 v[188:189], 0\n\t" \
  "v_mov_b64 v[190:191], 0\n\t" \
  "v_mov_b64 v[192:193], 0\n\t" \
  "v_mov_b64 v[194:195], 0\n\t" \
  "v_mov_b64 v[196:197], 0\n\t" \
  "v_mov_b64 v[198:199], 0\n\t" \
  "v_mov_b64 v[200:201], 0\n\t" \
  "v_mov_b64 v[202:203], 0\n\t" \
  "v_mov_b64 v[204:205], 0\n\t" \
  "v_mov_b64 v[206:207], 0\n\t" \
  "v_mov_b64 v[208:209], 0\n\t" \
  "v_mov_b64 v[210:211], 0\n\t" \
  "v_mov_b64 v[212:213], 0\n\t" \
  "v_mov_b64 v[214:215], 0\n\t" \
  "v_mov_b64 v[216:217], 0\n\t" \
  "v_mov_b64 v[218:219], 0\n\t" \
  "v_mov_b64 v[220:221], 0\n\t" \
  "v_mov_b64 v[222:223], 0\n\t" \
  "s_mov_b64 %[last], 0\n\t" \
  "s_mov_b32 %[dl], %[pos]\n\t" \
  "s_mov_b32 %[cnt], 0\n\t" \
  "s_mov_b32 %[sl], 0\n\t" \
  "s_branch 3f\n\t" \
  "4:\n\t" \
  "s_sleep 1\n\t" \
  "s_lshr_b64 %[tmp], %[tmp], 1\n\t" \
  "s_cbranch_scc1 4b\n\t" \
  "s_branch 5f\n\t" \
  "3:\n\t" \
  "s_barrier\n\t" \
  "ds_read_b128 v[48:51], %[a0]\n\t" \
  "ds_read_b64_tr_b16 v[64:65], %[b0] offset:0\n\t" \
  "ds_read_b64_tr_b16 v[66:67], %[b0] offset:512\n\t" \
  "ds_read_b128 v[52:55], %[a0] offset:2048\n\t" \
  "ds_read_b128 v[56:59], %[a0] offset:4096\n\t" \
  "ds_read_b128 v[60:63], %[a0] offset:6144\n\t" \
  "ds_read_b64_tr_b16 v[68:69], %[b1] offset:0\n\t" \
  "ds_read_b64_tr_b16 v[70:71], %[b1] offset:512\n\t" \
  "ds_read_b64_tr_b16 v[72:73], %[b0] offset:8192\n\t" \
  "ds_read_b64_tr_b16 v[74:75], %[b0] offset:8704\n\t" \
  "ds_read_b64_tr_b16 v[76:77], %[b1] offset:8192\n\t" \
  "ds_read_b64_tr_b16 v[78:79], %[b1] offset:8704\n\t" \
  "ds_read_b64_tr_b16 v[80:81], %[b0] offset:16384\n\t" \
  "ds_read_b64_tr_b16 v[82:83], %[b0] offset:16896\n\t" \
  "ds_read_b64_tr_b16 v[84:85], %[b1] offset:16384\n\t" \
  "ds_read_b64_tr_b16 v[86:87], %[b1] offset:16896\n\t" \
  ".p2align 5\n\t" \
  "2:\n\t" \
  "s_waitcnt lgkmcnt(13)\n\t" \
  "v_mfma_f32_16x16x32_bf16 v[128:131], v[64:67], v[48:51], v[128:131]\n\t" \
  "ds_read_b128 v[88:91], %[a1]\n\t" \
  "s_waitcnt lgkmcnt(13)\n\t" \
  "v_mfma_f32_16x16x32_bf16 v[132:135], v[64:67], v[52:55], v[132:135]\n\t" \
  "ds_read_b64_tr_b16 v[104:105], %[b0] offset:4096\n\t" \
  "s_waitcnt lgkmcnt(13)\n\t" \
  "v_mfma_f32_16x16x32_bf16 v[136:139], v[64:67], v[56:59], v[136:139]\n\t" \
  "ds_read_b64_tr_b16 v[106:107], %[b0] offset:4608\n\t" \
  "s_waitcnt lgkmcnt(13)\n\t" \
  "v_mfma_f32_16x16x32_bf16 v[140:143], v[64:67], v[60:63], v[140:143]\n\t" \
  "ds_read_b128 v[92:95], %[a1] offset:2048\n\t" \
  "s_waitcnt lgkmcnt(12)\n\t" \
  "v_mfma_f32_16x16x32_bf16 v[156:159], v[68:71], v[60:63], v[156:159]\n\t" \
  "ds_read_b128 v[96:99], %[a1] offset:4096\n\t" \
  "v_mfma_f32_16x16x32_bf16 v[152:155], v[68:71], v[56:59], v[152:155]\n\t" \
  "ds_read_b128 v[100:103], %[a1] offset:6144\n\t" \
  "v_mfma_f32_16x16x32_bf16 v[148:151], v[68:71], v[52:55], v[148:151]\n\t" \
  "ds_read_b64_tr_b16 v[108:109], %[b1] offset:4096\n\t" \
  "v_mfma_f32_16x16x32_bf16 v[144:147], v[68:71], v[48:51], v[144:147]\n\t" \
  "ds_read_b64_tr_b16 v[110:111], %[b1] offset:4608\n\t" \
  "s_waitcnt lgkmcnt(14)\n\t" \
  "v_mfma_f32_16x16x32_bf16 v[160:163], v[72:75], v[48:51], v[160:163]\n\t" \
  "ds_read_b64_tr_b16 v[112:113], %[b0] offset:12288\n\t" \
  "v_mfma_f32_16x16x32_bf16 v[164:167], v[72:75], v[52:55], v[164:167]\n\t" \
  "ds_read_b64_tr_b16 v[114:115], %[b0] offset:12800\n\t" \
  "v_mfma_f32_16x16x32_bf16 v[168:171], v[72:75], v[56:59], v[168:171]\n\t" \
  "ds_read_b64_tr_b16 v[116:117], %[b1] offset:12288\n\t" \
  "v_mfma_f32_16x16x32_bf16 v[172:175], v[72:75], v[60:63], v[172:175]\n\t" \
  "ds_read_b64_tr_b16 v[118:119], %[b1] offset:12800\n\t" \
  "s_waitcnt lgkmcnt(14)\n\t" \
  "v_mfma_f32_16x16x32_bf16 v[188:191], v[76:79], v[60:63], v[188:191]\n\t" \
  "ds_read_b64_tr_b16 v[120:121], %[b0] offset:20480\n\t" \
  "v_mfma_f32_16x16x32_bf16 v[184:187], v[76:79], v[56:59], v[184:187]\n\t" \
  "ds_read_b64_tr_b16 v[122:123], %[b0] offset:20992\n\t" \
  "v_mfma_f32_16x16x32_bf16 v[180:183], v[76:79], v[52:55], v[180:183]\n\t" \
  "ds_read_b64_tr_b16 v[124:125], %[b1] offset:20480\n\t" \
  "v_mfma_f32_16x16x32_bf16 v[176:179], v[76:79], v[48:51], v[176:179]\n\t" \
  "ds_read_b64_tr_b16 v[126:127], %[b1] offset:20992\n\t" \
  "s_waitcnt lgkmcnt(14)\n\t" \
  "v_mfma_f32_16x16x32_bf16 v[192:195], v[48:51], v[80:83], v[192:195]\n\t" \
  "v_add_u32 %[a0], %[dl], %[a0]\n\t" \
  "s_and_b64 %[tmp], %[ramp], 15\n\t" \
  "v_mfma_f32_16x16x32_bf16 v[196:199], v[52:55], v[80:83], v[196:199]\n\t" \
  "v_add_u32 %[b0], %[dl], %[b0]\n\t" \
  "s_cbranch_scc1 4b\n\t" \
  "5:\n\t" \
  "v_mfma_f32_16x16x32_bf16 v[200:203], v[56:59], v[80:83], v[200:203]\n\t" \
  "v_add_u32 %[b1], %[dl], %[b1]\n\t" \
  "s_lshr_b64 %[ramp], %[ramp], 4\n\t" \
  "v_mfma_f32_16x16x32_bf16 v[204:207], v[60:63], v[80:83], v[204:207]\n\t" \
  "v_add_u32 %[a1], %[dl], %[a1]\n\t" \
  "s_waitcnt lgkmcnt(14)\n\t" \
  "v_mfma_f32_16x16x32_bf16 v[220:223], v[60:63], v[84:87], v[220:223]\n\t" \
  "v_cndmask_b32 %[b0], %[b0], %[w0], %[last]\n\t" \
  "v_mfma_f32_16x16x32_bf16 v[216:219], v[56:59], v[84:87], v[216:219]\n\t" \
  "v_cndmask_b32 %[b1], %[b1], %[w1], %[last]\n\t" \
  "v_mfma_f32_16x16x32_bf16 v[212:215], v[52:55], v[84:87], v[212:215]\n\t" \
  "v_mfma_f32_16x16x32_bf16 v[208:211], v[48:51], v[84:87], v[208:211]\n\t" \
  "s_waitcnt lgkmcnt(0)\n\t" \
  "s_barrier\n\t" \
  "v_mfma_f32_16x16x32_bf16 v[128:131], v[104:107], v[88:91], v[128:131]\n\t" \
  "ds_read_b128 v[48:51], %[a0]\n\t" \
  "v_mfma_f32_16x16x32_bf16 v[132:135], v[104:107], v[92:95], v[132:135]\n\t" \
  "ds_read_b64_tr_b16 v[64:65], %[b0] offset:0\n\t" \
  "v_mfma_f32_16x16x32_bf16 v[136:139], v[104:107], v[96:99], v[136:139]\n\t" \
  "ds_read_b64_tr_b16 v[66:67], %[b0] offset:512\n\t" \
  "v_mfma_f32_16x16x32_bf16 v[140:143], v[104:107], v[100:103], v[140:143]\n\t" \
  "ds_read_b128 v[52:55], %[a0] offset:2048\n\t" \
  "v_mfma_f32_16x16x32_bf16 v[156:159], v[108:111], v[100:103], v[156:159]\n\t" \
  "ds_read_b128 v[56:59], %[a0] offset:4096\n\t" \
  "v_mfma_f32_16x16x32_bf16 v[152:155], v[108:111], v[96:99], v[152:155]\n\t" \
  "ds_read_b128 v[60:63], %[a0] offset:6144\n\t" \
  "v_mfma_f32_16x16x32_bf16 v[148:151], v[108:111], v[92:95], v[148:151]\n\t" \
  "ds_read_b64_tr_b16 v[68:69], %[b1] offset:0\n\t" \
  "v_mfma_f32_16x16x32_bf16 v[144:147], v[108:111], v[88:91], v[144:147]\n\t" \
  "ds_read_b64_tr_b16 v[70:71], %[b1] offset:512\n\t" \
  "v_mfma_f32_16x16x32_bf16 v[160:163], v[112:115], v[88:91], v[160:163]\n\t" \
  "ds_read_b64_tr_b16 v[72:73], %[b0] offset:8192\n\t" \
  "v_mfma_f32_16x16x32_bf16 v[164:167], v[112:115], v[92:95], v[164:167]\n\t" \
  "ds_read_b64_tr_b16 v[74:75], %[b0] offset:8704\n\t" \
  "v_mfma_f32_16x16x32_bf16 v[168:171], v[112:115], v[96:99], v[168:171]\n\t" \
  "ds_read_b64_tr_b16 v[76:77], %[b1] offset:8192\n\t" \
  "v_mfma_f32_16x16x32_bf16 v[172:175], v[112:115], v[100:103], v[172:175]\n\t" \
  "ds_read_b64_tr_b16 v[78:79], %[b1] offset:8704\n\t" \
  "v_mfma_f32_16x16x32_bf16 v[188:191], v[116:119], v[100:103], v[188:191]\n\t" \
  "ds_read_b64_tr_b16 v[80:81], %[b0] offset:16384\n\t" \
  "v_mfma_f32_16x16x32_bf16 v[184:187], v[116:119], v[96:99], v[184:187]\n\t" \
  "ds_read_b64_tr_b16 v[82:83], %[b0] offset:16896\n\t" \
  "v_mfma_f32_16x16x32_bf16 v[180:183], v[116:119], v[92:95], v[180:183]\n\t" \
  "ds_read_b64_tr_b16 v[84:85], %[b1] offset:16384\n\t" \
  "v_mfma_f32_16x16x32_bf16 v[176:179], v[116:119], v[88:91], v[176:179]\n\t" \
  "ds_read_b64_tr_b16 v[86:87], %[b1] offset:16896\n\t" \
  "v_mfma_f32_16x16x32_bf16 v[192:195], v[88:91], v[120:123], v[192:195]\n\t" \
  "s_add_u32 %[sl], %[sl], 1\n\t" \
  "v_mfma_f32_16x16x32_bf16 v[196:199], v[92:95], v[120:123], v[196:199]\n\t" \
  "s_cmp_eq_u32 %[sl], 2\n\t" \
  "v_mfma_f32_16x16x32_bf16 v[200:203], v[96:99], v[120:123], v[200:203]\n\t" \
  "s_cselect_b32 %[dl], %[neg], %[pos]\n\t" \
  "v_mfma_f32_16x16x32_bf16 v[204:207], v[100:103], v[120:123], v[204:207]\n\t" \
  "s_cselect_b32 %[sl], -1, %[sl]\n\t" \
  "v_mfma_f32_16x16x32_bf16 v[220:223], v[100:103], v[124:127], v[220:223]\n\t" \
  "s_add_u32 %[cnt], %[cnt], 1\n\t" \
  "v_mfma_f32_16x16x32_bf16 v[216:219], v[96:99], v[124:127], v[216:219]\n\t" \
  "s_cmp_eq_u32 %[cnt], 15\n\t" \
  "v_mfma_f32_16x16x32_bf16 v[212:215], v[92:95], v[124:127], v[212:215]\n\t" \
  "s_cselect_b64 %[last], -1, 0\n\t" \
  "s_cmp_lg_u32 %[cnt], 16\n\t" \
  "v_mfma_f32_16x16x32_bf16 v[208:211], v[88:91], v[124:127], v[208:211]\n\t" \
  "s_cbranch_scc1 2b\n\t" \
  "s_waitcnt lgkmcnt(0)\n\t" \
  "s_nop 6\n\t"

#define KMAP_ASM \
  "v_cvt_pk_bf16_f32 v48, v160, v161\n\t" \
  "v_cvt_pk_bf16_f32 v49, v162, v163\n\t" \
  "v_cvt_pk_bf16_f32 v50, v176, v177\n\t" \
  "v_cvt_pk_bf16_f32 v51, v178, v179\n\t" \
  "v_cvt_pk_bf16_f32 v52, v164, v165\n\t" \
  "v_cvt_pk_bf16_f32 v53, v166, v167\n\t" \
  "v_cvt_pk_bf16_f32 v54, v180, v181\n\t" \
  "v_cvt_pk_bf16_f32 v55, v182, v183\n\t" \
  "v_permlane16_swap_b32 v48, v52\n\t" \
  "v_permlane16_swap_b32 v49, v53\n\t" \
  "v_permlane16_swap_b32 v50, v54\n\t" \
  "v_permlane16_swap_b32 v51, v55\n\t" \
  "v_cvt_pk_bf16_f32 v56, v168, v169\n\t" \
  "v_cvt_pk_bf16_f32 v57, v170, v171\n\t" \
  "v_mfma_f32_32x32x16_bf16 v[72:87], v[64:67], v[48:51], 0\n\t" \
  "v_cvt_pk_bf16_f32 v58, v184, v185\n\t" \
  "v_cvt_pk_bf16_f32 v59, v186, v187\n\t" \
  "v_cvt_pk_bf16_f32 v60, v172, v173\n\t" \
  "v_cvt_pk_bf16_f32 v61, v174, v175\n\t" \
  "v_cvt_pk_bf16_f32 v62, v188, v189\n\t" \
  "v_cvt_pk_bf16_f32 v63, v190, v191\n\t" \
  "v_mfma_f32_32x32x16_bf16 v[72:87], v[68:71], v[52:55], v[72:87]\n\t" \
  "v_permlane16_swap_b32 v56, v60\n\t" \
  "v_permlane16_swap_b32 v57, v61\n\t" \
  "v_permlane16_swap_b32 v58, v62\n\t" \
  "v_permlane16_swap_b32 v59, v63\n\t" \
  "v_cvt_pk_bf16_f32 v192, v192, v193\n\t" \
  "v_cvt_pk_bf16_f32 v193, v194, v195\n\t" \
  "v_mfma_f32_32x32x16_bf16 v[88:103], v[64:67], v[56:59], 0\n\t" \
  "v_cvt_pk_bf16_f32 v194, v196, v197\n\t" \
  "v_cvt_pk_bf16_f32 v195, v198, v199\n\t" \
  "ds_write_b128 %[vf], v[192:195] offset:0\n\t" \
  "v_cvt_pk_bf16_f32 v208, v208, v209\n\t" \
  "v_cvt_pk_bf16_f32 v209, v210, v211\n\t" \
  "v_cvt_pk_bf16_f32 v210, v212, v213\n\t" \
  "v_mfma_f32_32x32x16_bf16 v[88:103], v[68:71], v[60:63], v[88:103]\n\t" \
  "v_cvt_pk_bf16_f32 v211, v214, v215\n\t" \
  "ds_write_b128 %[vf], v[208:211] offset:256\n\t" \
  "v_max3_f32 v104, v72, v73, v73\n\t" \
  "v_max3_f32 v105, -v72, -v73, -v73\n\t" \
  "v_cvt_pk_bf16_f32 v200, v200, v201\n\t" \
  "v_max3_f32 v104, v74, v75, v104\n\t" \
  "v_max3_f32 v105, -v74, -v75, v105\n\t" \
  "v_cvt_pk_bf16_f32 v201, v202, v203\n\t" \
  "v_max3_f32 v104, v76, v77, v104\n\t" \
  "v_max3_f32 v105, -v76, -v77, v105\n\t" \
  "v_cvt_pk_bf16_f32 v202, v204, v205\n\t" \
  "v_max3_f32 v104, v78, v79, v104\n\t" \
  "v_max3_f32 v105, -v78, -v79, v105\n\t" \
  "v_cvt_pk_bf16_f32 v203, v206, v207\n\t" \
  "v_max3_f32 v104, v80, v81, v104\n\t" \
  "v_max3_f32 v105, -v80, -v81, v105\n\t" \
  "ds_write_b128 %[vf], v[200:203] offset:2048\n\t" \
  "v_max3_f32 v104, v82, v83, v104\n\t" \
  "v_max3_f32 v105, -v82, -v83, v105\n\t" \
  "v_cvt_pk_bf16_f32 v216, v216, v217\n\t" \
  "v_max3_f32 v104, v84, v85, v104\n\t" \
  "v_max3_f32 v105, -v84, -v85, v105\n\t" \
  "v_cvt_pk_bf16_f32 v217, v218, v219\n\t" \
  "v_max3_f32 v104, v86, v87, v104\n\t" \
  "v_max3_f32 v105, -v86, -v87, v105\n\t" \
  "v_cvt_pk_bf16_f32 v218, v220, v221\n\t" \
  "v_cvt_pk_bf16_f32 v219, v222, v223\n\t" \
  "ds_write_b128 %[vf], v[216:219] offset:2304\n\t" \
  "v_max3_f32 v108, v88, v89, v89\n\t" \
  "v_max3_f32 v109, -v88, -v89, -v89\n\t" \
  "v_permlane32_swap_b32 v104, v105\n\t" \
  "v_max3_f32 v108, v90, v91, v108\n\t" \
  "v_max3_f32 v109, -v90, -v91, v109\n\t" \
  "v_max3_f32 v108, v92, v93, v108\n\t" \
  "v_max3_f32 v109, -v92, -v93, v109\n\t" \
  "v_max_f32 v105, v104, v105\n\t" \
  "v_max3_f32 v108, v94, v95, v108\n\t" \
  "v_max3_f32 v109, -v94, -v95, v109\n\t" \
  "v_max3_f32 v108, v96, v97, v108\n\t" \
  "v_max3_f32 v109, -v96, -v97, v109\n\t" \
  "v_mul_f32 v104, %[nc], v105\n\t" \
  "v_max3_f32 v108, v98, v99, v108\n\t" \
  "v_max3_f32 v109, -v98, -v99, v109\n\t" \
  "v_max3_f32 v108, v100, v101, v108\n\t" \
  "v_max3_f32 v109, -v100, -v101, v109\n\t" \
  "v_mul_f32 v106, %[nc], v105\n\t" \
  "v_max3_f32 v108, v102, v103, v108\n\t" \
  "v_max3_f32 v109, -v102, -v103, v109\n\t" \
  "v_xor_b32 v121, 32, %[pk]\n\t" \
  "v_xor_b32 v122, 64, %[pk]\n\t" \
  "v_permlane32_swap_b32 v104, v106\n\t" \
  "v_pk_fma_f32 v[160:161], v[72:73], %[cc], v[106:107] op_sel_hi:[1,0,0] neg_lo:[0,1,0] neg_hi:[0,1,0]\n\t" \
  "v_pk_fma_f32 v[72:73], v[72:73], %[cc], v[104:105] op_sel_hi:[1,0,0]\n\t" \
  "v_permlane32_swap_b32 v108, v109\n\t" \
  "v_exp_f32 v160, v160\n\t" \
  "v_pk_fma_f32 v[162:163], v[74:75], %[cc], v[106:107] op_sel_hi:[1,0,0] neg_lo:[0,1,0] neg_hi:[0,1,0]\n\t" \
  "v_exp_f32 v161, v161\n\t" \
  "v_max_f32 v109, v108, v109\n\t" \
  "v_exp_f32 v72, v72\n\t" \
  "v_pk_fma_f32 v[74:75], v[74:75], %[cc], v[104:105] op_sel_hi:[1,0,0]\n\t" \
  "v_exp_f32 v73, v73\n\t" \
  "v_mul_f32 v108, %[nc], v109\n\t" \
  "v_exp_f32 v162, v162\n\t" \
  "v_pk_fma_f32 v[164:165], v[76:77], %[cc], v[106:107] op_sel_hi:[1,0,0] neg_lo:[0,1,0] neg_hi:[0,1,0]\n\t" \
  "v_exp_f32 v163, v163\n\t" \
  "v_mul_f32 v110, %[nc], v109\n\t" \
  "v_exp_f32 v74, v74\n\t" \
  "v_pk_fma_f32 v[76:77], v[76:77], %[cc], v[104:105] op_sel_hi:[1,0,0]\n\t" \
  "v_exp_f32 v75, v75\n\t" \
  "v_permlane32_swap_b32 v108, v110\n\t" \
  "v_exp_f32 v164, v164\n\t" \
  "v_pk_fma_f32 v[166:167], v[78:79], %[cc], v[106:107] op_sel_hi:[1,0,0] neg_lo:[0,1,0] neg_hi:[0,1,0]\n\t" \
  "v_exp_f32 v165, v165\n\t" \
  "v_xor_b32 v123, 96, %[pk]\n\t" \
  "v_pk_add_f32 v[114:115], v[162:163], v[160:161]\n\t" \
  "v_exp_f32 v76, v76\n\t" \
  "v_pk_fma_f32 v[78:79], v[78:79], %[cc], v[104:105] op_sel_hi:[1,0,0]\n\t" \
  "v_exp_f32 v77, v77\n\t" \
  "v_pk_add_f32 v[112:113], v[74:75], v[72:73]\n\t" \
  "v_exp_f32 v166, v166\n\t" \
  "v_pk_fma_f32 v[168:169], v[80:81], %[cc], v[106:107] op_sel_hi:[1,0,0] neg_lo:[0,1,0] neg_hi:[0,1,0]\n\t" \
  "v_exp_f32 v167, v167\n\t" \
  "v_pk_add_f32 v[114:115], v[164:165], v[114:115]\n\t" \
  "v_exp_f32 v78, v78\n\t" \
  "v_pk_fma_f32 v[80:81], v[80:81], %[cc], v[104:105] op_sel_hi:[1,0,0]\n\t" \
  "v_exp_f32 v79, v79\n\t" \
  "v_pk_add_f32 v[112:113], v[76:77], v[112:113]\n\t" \
  "v_exp_f32 v168, v168\n\t" \
  "v_pk_fma_f32 v[170:171], v[82:83], %[cc], v[106:107] op_sel_hi:[1,0,0] neg_lo:[0,1,0] neg_hi:[0,1,0]\n\t" \
  "v_exp_f32 v169, v169\n\t" \
  "v_pk_add_f32 v[114:115], v[166:167], v[114:115]\n\t" \
  "v_exp_f32 v80, v80\n\t" \
  "v_pk_fma_f32 v[82:83], v[82:83], %[cc], v[104:105] op_sel_hi:[1,0,0]\n\t" \
  "v_exp_f32 v81, v81\n\t" \
  "v_pk_add_f32 v[112:113], v[78:79], v[112:113]\n\t" \
  "v_exp_f32 v170, v170\n\t" \
  "v_pk_fma_f32 v[172:173], v[84:85], %[cc], v[106:107] op_sel_hi:[1,0,0] neg_lo:[0,1,0] neg_hi:[0,1,0]\n\t" \
  "v_exp_f32 v171, v171\n\t" \
  "v_pk_add_f32 v[114:115], v[168:169], v[114:115]\n\t" \
  "v_exp_f32 v82, v82\n\t" \
  "v_pk_fma_f32 v[84:85], v[84:85], %[cc], v[104:105] op_sel_hi:[1,0,0]\n\t" \
  "v_exp_f32 v83, v83\n\t" \
  "v_pk_add_f32 v[112:113], v[80:81], v[112:113]\n\t" \
  "v_exp_f32 v172, v172\n\t" \
  "v_pk_fma_f32 v[174:175], v[86:87], %[cc], v[106:107] op_sel_hi:[1,0,0] neg_lo:[0,1,0] neg_hi:[0,1,0]\n\t" \
  "v_exp_f32 v173, v173\n\t" \
  "v_pk_add_f32 v[114:115], v[170:171], v[114:115]\n\t" \
  "v_exp_f32 v84, v84\n\t" \
  "v_pk_fma_f32 v[86:87], v[86:87], %[cc], v[104:105] op_sel_hi:[1,0,0]\n\t" \
  "v_exp_f32 v85, v85\n\t" \
  "v_pk_add_f32 v[112:113], v[82:83], v[112:113]\n\t" \
  "v_exp_f32 v174, v174\n\t" \
  "v_exp_f32 v175, v175\n\t" \
  "v_pk_add_f32 v[114:115], v[172:173], v[114:115]\n\t" \
  "v_exp_f32 v86, v86\n\t" \
  "v_exp_f32 v87, v87\n\t" \
  "v_pk_add_f32 v[112:113], v[84:85], v[112:113]\n\t" \
  "v_pk_add_f32 v[114:115], v[174:175], v[114:115]\n\t" \
  "v_pk_add_f32 v[112:113], v[86:87], v[112:113]\n\t" \
  "v_pk_fma_f32 v[176:177], v[88:89], %[cc], v[110:111] op_sel_hi:[1,0,0] neg_lo:[0,1,0] neg_hi:[0,1,0]\n\t" \
  "v_pk_fma_f32 v[88:89], v[88:89], %[cc], v[108:109] op_sel_hi:[1,0,0]\n\t" \
  "v_add_f32 v114, v114, v115\n\t" \
  "v_exp_f32 v176, v176\n\t" \
  "v_pk_fma_f32 v[178:179], v[90:91], %[cc], v[110:111] op_sel_hi:[1,0,0] neg_lo:[0,1,0] neg_hi:[0,1,0]\n\t" \
  "v_exp_f32 v177, v177\n\t" \
  "v_add_f32 v112, v112, v113\n\t" \
  "v_exp_f32 v88, v88\n\t" \
  "v_pk_fma_f32 v[90:91], v[90:91], %[cc], v[108:109] op_sel_hi:[1,0,0]\n\t" \
  "v_exp_f32 v89, v89\n\t" \
  "v_permlane32_swap_b32 v112, v114\n\t" \
  "v_exp_f32 v178, v178\n\t" \
  "v_pk_fma_f32 v[180:181], v[92:93], %[cc], v[110:111] op_sel_hi:[1,0,0] neg_lo:[0,1,0] neg_hi:[0,1,0]\n\t" \
  "v_exp_f32 v179, v179\n\t" \
  "v_add_f32 v113, v112, v114\n\t" \
  "v_exp_f32 v90, v90\n\t" \
  "v_pk_fma_f32 v[92:93], v[92:93], %[cc], v[108:109] op_sel_hi:[1,0,0]\n\t" \
  "v_exp_f32 v91, v91\n\t" \
  "v_rcp_f32 v112, v113\n\t" \
  "v_exp_f32 v180, v180\n\t" \
  "v_pk_fma_f32 v[182:183], v[94:95], %[cc], v[110:111] op_sel_hi:[1,0,0] neg_lo:[0,1,0] neg_hi:[0,1,0]\n\t" \
  "v_exp_f32 v181, v181\n\t" \
  "v_mov_b32 v114, v112\n\t" \
  "v_pk_add_f32 v[118:119], v[178:179], v[176:177]\n\t" \
  "v_exp_f32 v92, v92\n\t" \
  "v_pk_fma_f32 v[94:95], v[94:95], %[cc], v[108:109] op_sel_hi:[1,0,0]\n\t" \
  "v_permlane32_swap_b32 v112, v114\n\t" \
  "v_exp_f32 v93, v93\n\t" \
  "v_pk_add_f32 v[116:117], v[90:91], v[88:89]\n\t" \
  "v_exp_f32 v182, v182\n\t" \
  "v_pk_fma_f32 v[184:185], v[96:97], %[cc], v[110:111] op_sel_hi:[1,0,0] neg_lo:[0,1,0] neg_hi:[0,1,0]\n\t" \
  "v_exp_f32 v183, v183\n\t" \
  "v_pk_add_f32 v[118:119], v[180:181], v[118:119]\n\t" \
  "v_exp_f32 v94, v94\n\t" \
  "v_pk_fma_f32 v[96:97], v[96:97], %[cc], v[108:109] op_sel_hi:[1,0,0]\n\t" \
  "v_exp_f32 v95, v95\n\t" \
  "v_pk_add_f32 v[116:117], v[92:93], v[116:117]\n\t" \
  "v_exp_f32 v184, v184\n\t" \
  "v_pk_fma_f32 v[186:187], v[98:99], %[cc], v[110:111] op_sel_hi:[1,0,0] neg_lo:[0,1,0] neg_hi:[0,1,0]\n\t" \
  "v_exp_f32 v185, v185\n\t" \
  "v_pk_add_f32 v[118:119], v[182:183], v[118:119]\n\t" \
  "v_exp_f32 v96, v96\n\t" \
  "v_pk_fma_f32 v[98:99], v[98:99], %[cc], v[108:109] op_sel_hi:[1,0,0]\n\t" \
  "v_exp_f32 v97, v97\n\t" \
  "v_pk_add_f32 v[116:117], v[94:95], v[116:117]\n\t" \
  "v_exp_f32 v186, v186\n\t" \
  "v_pk_fma_f32 v[188:189], v[100:101], %[cc], v[110:111] op_sel_hi:[1,0,0] neg_lo:[0,1,0] neg_hi:[0,1,0]\n\t" \
  "v_exp_f32 v187, v187\n\t" \
  "v_pk_add_f32 v[118:119], v[184:185], v[118:119]\n\t" \
  "v_exp_f32 v98, v98\n\t" \
  "v_pk_fma_f32 v[100:101], v[100:101], %[cc], v[108:109] op_sel_hi:[1,0,0]\n\t" \
  "v_exp_f32 v99, v99\n\t" \
  "v_pk_add_f32 v[116:117], v[96:97], v[116:117]\n\t" \
  "v_exp_f32 v188, v188\n\t" \
  "v_pk_fma_f32 v[190:191], v[102:103], %[cc], v[110:111] op_sel_hi:[1,0,0] neg_lo:[0,1,0] neg_hi:[0,1,0]\n\t" \
  "v_exp_f32 v189, v189\n\t" \
  "v_pk_add_f32 v[118:119], v[186:187], v[118:119]\n\t" \
  "v_exp_f32 v100, v100\n\t" \
  "v_pk_fma_f32 v[102:103], v[102:103], %[cc], v[108:109] op_sel_hi:[1,0,0]\n\t" \
  "v_exp_f32 v101, v101\n\t" \
  "v_pk_add_f32 v[116:117], v[98:99], v[116:117]\n\t" \
  "v_exp_f32 v190, v190\n\t" \
  "v_exp_f32 v191, v191\n\t" \
  "v_pk_add_f32 v[118:119], v[188:189], v[118:119]\n\t" \
  "v_exp_f32 v102, v102\n\t" \
  "v_exp_f32 v103, v103\n\t" \
  "v_pk_add_f32 v[116:117], v[100:101], v[116:117]\n\t" \
  "v_pk_add_f32 v[118:119], v[190:191], v[118:119]\n\t" \
  "v_pk_add_f32 v[116:117], v[102:103], v[116:117]\n\t" \
  "v_pk_mul_f32 v[72:73], v[72:73], v[112:113] op_sel_hi:[1,0]\n\t" \
  "v_add_f32 v118, v118, v119\n\t" \
  "v_pk_mul_f32 v[74:75], v[74:75], v[112:113] op_sel_hi:[1,0]\n\t" \
  "v_pk_mul_f32 v[76:77], v[76:77], v[112:113] op_sel_hi:[1,0]\n\t" \
  "v_add_f32 v116, v116, v117\n\t" \
  "v_pk_mul_f32 v[78:79], v[78:79], v[112:113] op_sel_hi:[1,0]\n\t" \
  "v_cvt_pk_bf16_f32 v72, v72, v73\n\t" \
  "v_permlane32_swap_b32 v116, v118\n\t" \
  "v_cvt_pk_bf16_f32 v73, v74, v75\n\t" \
  "v_cvt_pk_bf16_f32 v74, v76, v77\n\t" \
  "v_add_f32 v117, v116, v118\n\t" \
  "v_cvt_pk_bf16_f32 v75, v78, v79\n\t" \
  "ds_write_b128 %[pk], v[72:75] offset:0\n\t" \
  "v_rcp_f32 v116, v117\n\t" \
  "v_pk_mul_f32 v[80:81], v[80:81], v[112:113] op_sel_hi:[1,0]\n\t" \
  "v_pk_mul_f32 v[82:83], v[82:83], v[112:113] op_sel_hi:[1,0]\n\t" \
  "v_mov_b32 v118, v116\n\t" \
  "v_pk_mul_f32 v[84:85], v[84:85], v[112:113] op_sel_hi:[1,0]\n\t" \
  "v_pk_mul_f32 v[86:87], v[86:87], v[112:113] op_sel_hi:[1,0]\n\t" \
  "v_permlane32_swap_b32 v116, v118\n\t" \
  "v_cvt_pk_bf16_f32 v80, v80, v81\n\t" \
  "v_cvt_pk_bf16_f32 v81, v82, v83\n\t" \
  "v_cvt_pk_bf16_f32 v82, v84, v85\n\t" \
  "v_cvt_pk_bf16_f32 v83, v86, v87\n\t" \
  "ds_write_b128 v121, v[80:83] offset:0\n\t" \
  "v_pk_mul_f32 v[160:161], v[160:161], v[114:115] op_sel_hi:[1,0]\n\t" \
  "v_pk_mul_f32 v[162:163], v[162:163], v[114:115] op_sel_hi:[1,0]\n\t" \
  "v_pk_mul_f32 v[164:165], v[164:165], v[114:115] op_sel_hi:[1,0]\n\t" \
  "v_pk_mul_f32 v[166:167], v[166:167], v[114:115] op_sel_hi:[1,0]\n\t" \
  "v_cvt_pk_bf16_f32 v160, v160, v161\n\t" \
  "v_cvt_pk_bf16_f32 v161, v162, v163\n\t" \
  "v_cvt_pk_bf16_f32 v162, v164, v165\n\t" \
  "v_cvt_pk_bf16_f32 v163, v166, v167\n\t" \
  "ds_write_b128 v122, v[160:163] offset:0\n\t" \
  "v_pk_mul_f32 v[168:169], v[168:169], v[114:115] op_sel_hi:[1,0]\n\t" \
  "v_pk_mul_f32 v[170:171], v[170:171], v[114:115] op_sel_hi:[1,0]\n\t" \
  "v_pk_mul_f32 v[172:173], v[172:173], v[114:115] op_sel_hi:[1,0]\n\t" \
  "v_pk_mul_f32 v[174:175], v[174:175], v[114:115] op_sel_hi:[1,0]\n\t" \
  "v_cvt_pk_bf16_f32 v168, v168, v169\n\t" \
  "v_cvt_pk_bf16_f32 v169, v170, v171\n\t" \
  "v_cvt_pk_bf16_f32 v170, v172, v173\n\t" \
  "v_cvt_pk_bf16_f32 v171, v174, v175\n\t" \
  "ds_write_b128 v123, v[168:171] offset:0\n\t" \
  "v_pk_mul_f32 v[88:89], v[88:89], v[116:117] op_sel_hi:[1,0]\n\t" \
  "v_pk_mul_f32 v[90:91], v[90:91], v[116:117] op_sel_hi:[1,0]\n\t" \
  "v_pk_mul_f32 v[92:93], v[92:93], v[116:117] op_sel_hi:[1,0]\n\t" \
  "v_pk_mul_f32 v[94:95], v[94:95], v[116:117] op_sel_hi:[1,0]\n\t" \
  "v_cvt_pk_bf16_f32 v88, v88, v89\n\t" \
  "v_cvt_pk_bf16_f32 v89, v90, v91\n\t" \
  "v_cvt_pk_bf16_f32 v90, v92, v93\n\t" \
  "v_cvt_pk_bf16_f32 v91, v94, v95\n\t" \
  "ds_write_b128 %[pk], v[88:91] offset:4096\n\t" \
  "v_pk_mul_f32 v[96:97], v[96:97], v[116:117] op_sel_hi:[1,0]\n\t" \
  "v_pk_mul_f32 v[98:99], v[98:99], v[116:117] op_sel_hi:[1,0]\n\t" \
  "v_pk_mul_f32 v[100:101], v[100:101], v[116:117] op_sel_hi:[1,0]\n\t" \
  "v_pk_mul_f32 v[102:103], v[102:103], v[116:117] op_sel_hi:[1,0]\n\t" \
  "v_cvt_pk_bf16_f32 v96, v96, v97\n\t" \
  "v_cvt_pk_bf16_f32 v97, v98, v99\n\t" \
  "v_cvt_pk_bf16_f32 v98, v100, v101\n\t" \
  "v_cvt_pk_bf16_f32 v99, v102, v103\n\t" \
  "ds_write_b128 v121, v[96:99] offset:4096\n\t" \
  "v_pk_mul_f32 v[176:177], v[176:177], v[118:119] op_sel_hi:[1,0]\n\t" \
  "v_pk_mul_f32 v[178:179], v[178:179], v[118:119] op_sel_hi:[1,0]\n\t" \
  "v_pk_mul_f32 v[180:181], v[180:181], v[118:119] op_sel_hi:[1,0]\n\t" \
  "v_pk_mul_f32 v[182:183], v[182:183], v[118:119] op_sel_hi:[1,0]\n\t" \
  "v_cvt_pk_bf16_f32 v176, v176, v177\n\t" \
  "v_cvt_pk_bf16_f32 v177, v178, v179\n\t" \
  "v_cvt_pk_bf16_f32 v178, v180, v181\n\t" \
  "v_cvt_pk_bf16_f32 v179, v182, v183\n\t" \
  "ds_write_b128 v122, v[176:179] offset:4096\n\t" \
  "v_pk_mul_f32 v[184:185], v[184:185], v[118:119] op_sel_hi:[1,0]\n\t" \
  "v_pk_mul_f32 v[186:187], v[186:187], v[118:119] op_sel_hi:[1,0]\n\t" \
  "v_pk_mul_f32 v[188:189], v[188:189], v[118:119] op_sel_hi:[1,0]\n\t" \
  "v_pk_mul_f32 v[190:191], v[190:191], v[118:119] op_sel_hi:[1,0]\n\t" \
  "v_cvt_pk_bf16_f32 v184, v184, v185\n\t" \
  "v_cvt_pk_bf16_f32 v185, v186, v187\n\t" \
  "v_cvt_pk_bf16_f32 v186, v188, v189\n\t" \
  "v_cvt_pk_bf16_f32 v187, v190, v191\n\t" \
  "ds_write_b128 v123, v[184:187] offset:4096\n\t" \
  "s_waitcnt lgkmcnt(0)\n\t" \
  "s_barrier\n\t" \
  "ds_read_b64_tr_b16 v[72:73], %[ta0] offset:0\n\t" \
  "ds_read_b64_tr_b16 v[74:75], %[ta0] offset:2048\n\t" \
  "ds_read_b128 v[160:163], %[vr] offset:0\n\t" \
  "ds_read_b64_tr_b16 v[76:77], %[ta1] offset:0\n\t" \
  "ds_read_b64_tr_b16 v[78:79], %[ta1] offset:2048\n\t" \
  "ds_read_b128 v[164:167], %[vr] offset:1024\n\t" \
  "s_waitcnt lgkmcnt(3)\n\t" \
  "v_mfma_f32_32x32x16_bf16 v[104:119], v[72:75], v[160:163], 0\n\t" \
  "ds_read_b64_tr_b16 v[80:81], %[ta0] offset:4096\n\t" \
  "ds_read_b64_tr_b16 v[82:83], %[ta0] offset:6144\n\t" \
  "ds_read_b128 v[168:171], %[vr] offset:2048\n\t" \
  "s_waitcnt lgkmcnt(3)\n\t" \
  "v_mfma_f32_32x32x16_bf16 v[104:119], v[76:79], v[164:167], v[104:119]\n\t" \
  "ds_read_b64_tr_b16 v[84:85], %[ta1] offset:4096\n\t" \
  "ds_read_b64_tr_b16 v[86:87], %[ta1] offset:6144\n\t" \
  "ds_read_b128 v[172:175], %[vr] offset:3072\n\t" \
  "s_waitcnt lgkmcnt(3)\n\t" \
  "v_mfma_f32_32x32x16_bf16 v[104:119], v[80:83], v[168:171], v[104:119]\n\t" \
  "ds_read_b64_tr_b16 v[88:89], %[ta0] offset:8192\n\t" \
  "ds_read_b64_tr_b16 v[90:91], %[ta0] offset:10240\n\t" \
  "ds_read_b128 v[176:179], %[vr] offset:4096\n\t" \
  "s_waitcnt lgkmcnt(3)\n\t" \
  "v_mfma_f32_32x32x16_bf16 v[104:119], v[84:87], v[172:175], v[104:119]\n\t" \
  "ds_read_b64_tr_b16 v[92:93], %[ta1] offset:8192\n\t" \
  "ds_read_b64_tr_b16 v[94:95], %[ta1] offset:10240\n\t" \
  "ds_read_b128 v[180:183], %[vr] offset:5120\n\t" \
  "s_waitcnt lgkmcnt(3)\n\t" \
  "v_mfma_f32_32x32x16_bf16 v[104:119], v[88:91], v[176:179], v[104:119]\n\t" \
  "ds_read_b64_tr_b16 v[96:97], %[ta0] offset:12288\n\t" \
  "ds_read_b64_tr_b16 v[98:99], %[ta0] offset:14336\n\t" \
  "ds_read_b128 v[184:187], %[vr] offset:6144\n\t" \
  "s_waitcnt lgkmcnt(3)\n\t" \
  "v_mfma_f32_32x32x16_bf16 v[104:119], v[92:95], v[180:183], v[104:119]\n\t" \
  "ds_read_b64_tr_b16 v[100:101], %[ta1] offset:12288\n\t" \
  "ds_read_b64_tr_b16 v[102:103], %[ta1] offset:14336\n\t" \
  "ds_read_b128 v[188:191], %[vr] offset:7168\n\t" \
  "s_waitcnt lgkmcnt(3)\n\t" \
  "v_mfma_f32_32x32x16_bf16 v[104:119], v[96:99], v[184:187], v[104:119]\n\t" \
  "ds_read_b32 v120, %[fl]\n\t" \
  "s_waitcnt lgkmcnt(1)\n\t" \
  "v_mfma_f32_32x32x16_bf16 v[104:119], v[100:103], v[188:191], v[104:119]\n\t" \
  "v_cvt_pk_bf16_f32 v48, v128, v129\n\t" \
  "v_cvt_pk_bf16_f32 v49, v130, v131\n\t" \
  "v_cvt_pk_bf16_f32 v50, v144, v145\n\t" \
  "v_cvt_pk_bf16_f32 v51, v146, v147\n\t" \
  "v_cvt_pk_bf16_f32 v52, v132, v133\n\t" \
  "v_cvt_pk_bf16_f32 v53, v134, v135\n\t" \
  "v_cvt_pk_bf16_f32 v54, v148, v149\n\t" \
  "v_cvt_pk_bf16_f32 v55, v150, v151\n\t" \
  "s_waitcnt lgkmcnt(0)\n\t" \
  "v_cmp_eq_u32 vcc, 0, v120\n\t" \
  "s_cbranch_vccz 2f\n\t" \
  "1:\n\t" \
  "ds_read_b32 v120, %[fl]\n\t" \
  "s_waitcnt lgkmcnt(0)\n\t" \
  "v_cmp_eq_u32 vcc, 0, v120\n\t" \
  "s_cbranch_vccnz 1b\n\t" \
  "2:\n\t" \
  "s_nop 0\n\t" \
  "v_cvt_pk_bf16_f32 v104, v104, v105\n\t" \
  "v_cvt_pk_bf16_f32 v105, v106, v107\n\t" \
  "v_cvt_pk_bf16_f32 v106, v108, v109\n\t" \
  "v_cvt_pk_bf16_f32 v107, v110, v111\n\t" \
  "v_min_u32 v104, -2, v104\n\t" \
  "v_min_u32 v105, -2, v105\n\t" \
  "v_min_u32 v106, -2, v106\n\t" \
  "v_min_u32 v107, -2, v107\n\t" \
  "buffer_store_dwordx4 v[104:107], %[kvo], %[rs], %[s0] offen sc1\n\t" \
  "v_cvt_pk_bf16_f32 v112, v112, v113\n\t" \
  "v_cvt_pk_bf16_f32 v113, v114, v115\n\t" \
  "v_cvt_pk_bf16_f32 v114, v116, v117\n\t" \
  "v_cvt_pk_bf16_f32 v115, v118, v119\n\t" \
  "v_min_u32 v112, -2, v112\n\t" \
  "v_min_u32 v113, -2, v113\n\t" \
  "v_min_u32 v114, -2, v114\n\t" \
  "v_min_u32 v115, -2, v115\n\t" \
  "buffer_store_dwordx4 v[112:115], %[kvo], %[rs], %[s1] offen sc1\n\t" \
  "s_barrier\n\t" \
  "v_permlane16_swap_b32 v48, v52\n\t" \
  "v_permlane16_swap_b32 v49, v53\n\t" \
  "v_permlane16_swap_b32 v50, v54\n\t" \
  "v_permlane16_swap_b32 v51, v55\n\t" \
  "v_cvt_pk_bf16_f32 v56, v136, v137\n\t" \
  "v_cvt_pk_bf16_f32 v57, v138, v139\n\t" \
  "v_cvt_pk_bf16_f32 v58, v152, v153\n\t" \
  "v_cvt_pk_bf16_f32 v59, v154, v155\n\t" \
  "v_cvt_pk_bf16_f32 v60, v140, v141\n\t" \
  "v_cvt_pk_bf16_f32 v61, v142, v143\n\t" \
  "v_cvt_pk_bf16_f32 v62, v156, v157\n\t" \
  "v_cvt_pk_bf16_f32 v63, v158, v159\n\t" \
  "v_permlane16_swap_b32 v56, v60\n\t" \
  "v_permlane16_swap_b32 v57, v61\n\t" \
  "v_permlane16_swap_b32 v58, v62\n\t" \
  "v_permlane16_swap_b32 v59, v63\n\t" \
  "s_nop 1\n\t"

#define KMAP_CLOB "v72", "v73", "v74", "v75", "v76", "v77", "v78", "v79", "v80", "v81", "v82", "v83", "v84", "v85", "v86", "v87", "v88", "v89", "v90", "v91", "v92", "v93", "v94", "v95", "v96", "v97", "v98", "v99", "v100", "v101", "v102", "v103", "v104", "v105", "v106", "v107", "v108", "v109", "v110", "v111", "v112", "v113", "v114", "v115", "v116", "v117", "v118", "v119", "v120", "v121", "v122", "v123", "v124", "v125", "v126", "v127"
#define G1_CLOB "v48", "v49", "v50", "v51", "v52", "v53", "v54", "v55", "v56", "v57", "v58", "v59", "v60", "v61", "v62", "v63", "v72", "v73", "v74", "v75", "v76", "v77", "v78", "v79", "v80", "v81", "v82", "v83", "v84", "v85", "v86", "v87", "v88", "v89", "v90", "v91", "v92", "v93", "v94", "v95", "v96", "v97", "v98", "v99", "v100", "v101", "v102", "v103", "v104", "v105", "v106", "v107", "v108", "v109", "v110", "v111", "v112", "v113", "v114", "v115", "v116", "v117", "v118", "v119", "v120", "v121", "v122", "v123", "v124", "v125", "v126", "v127"
typedef u64 u64x2 __attribute__((ext_vector_type(2)));
template <int OFF> DI u64x2 karg2() {
  u64x2 r; asm volatile("s_load_dwordx4 %0, %1, %2\n\ts_waitcnt lgkmcnt(0)" : "=&s"(r) : "s"(__builtin_amdgcn_kernarg_segment_ptr()), "n"(OFF)); return r;
}
DI void poll1(rsrc_t rs, unsigned voff, unsigned soff) {
  unsigned f, cnt;
  asm volatile("s_mov_b32 %1, 0\n1:\n\tbuffer_load_dword %0, %2, %3, %4 offen sc1\n\ts_add_u32 %1, %1, 1\n\ts_cmp_gt_u32 %1, %5\n\ts_waitcnt vmcnt(0)\n\t"
               "v_cmp_ne_u32 vcc, 1, %0\n\ts_cbranch_scc1 2f\n\ts_cbranch_vccnz 1b\n2:"
               : "=&v"(f), "=&s"(cnt) : "v"(voff), "s"(rs), "s"(soff), "n"(SPIN_CAP) : "memory", "vcc", "scc");
}
DI bool marked(u32x4 a, u32x4 b) { return __builtin_amdgcn_ballot_w64(umax(umax(umax(umax(umax(a.x, a.y), a.z), umax(a.w, b.x)), umax(b.y, b.z)), b.w) == ~0u) != 0; }
DI void reload(u32x4& a, u32x4& b, rsrc_t rs, unsigned voff, unsigned sa, unsigned sb) {
  asm volatile("buffer_load_dwordx4 %0, %2, %3, %4 offen sc1\n\tbuffer_load_dwordx4 %1, %2, %3, %5 offen sc1\n\ts_waitcnt vmcnt(0)" : "+v"(a), "+v"(b) : "v"(voff), "s"(rs), "s"(sa), "s"(sb) : "memory");
}
DI unsigned lds_ld(unsigned a) { return __hip_atomic_load((LDS3 unsigned*)(size_t)a, __ATOMIC_RELAXED, __HIP_MEMORY_SCOPE_WORKGROUP); }
DI void lds_st(unsigned a, unsigned v) { __hip_atomic_store((LDS3 unsigned*)(size_t)a, v, __ATOMIC_RELAXED, __HIP_MEMORY_SCOPE_WORKGROUP); }

constexpr u64 unary(u64 r) { u64 o = 0; for (int i = 0; i < 64; i += 4) { const int n = (r >> i) & 15; if (n) o |= 1ull << (i + n - 1); } return o; }
DI int blk_of1(int rt, int ct) { return ((rt & 7) + 8 * (ct & 3)) * 8 + (rt >> 3) + 2 * (ct >> 2); }
DI int blk_of2(int rt, int ct) { return ((rt & 3) + 4 * (ct & 7)) * 8 + (rt >> 2) + 4 * (ct >> 3); }

struct Ids {
  int b, bi, rt, ct, rt2, ct2, cw, wr, hp, lane, r, h, q, p, blk, G, l15;
  DI Ids(int wave) {
    int t = threadIdx.x; b = blockIdx.x;
    asm volatile("" : "+v"(t)); asm volatile("" : "+s"(b), "+s"(wave));
    const int xcd = b & 7; bi = b >> 3;
    rt = (xcd & 1) * 8 + (bi & 7); ct = (xcd >> 1) * 4 + (bi >> 3);
    rt2 = (xcd & 3) * 4 + (bi & 3); ct2 = (xcd >> 2) * 8 + (bi >> 2);
    cw = wave & 3; wr = cw >> 1; hp = cw & 1;
    lane = t & 63; r = t & 31; h = lane >> 5; q = (t & 15) >> 2; p = t & 3; blk = (t >> 4) & 1; G = lane >> 4; l15 = t & 15;
  }
  DI unsigned hord() const {
    constexpr unsigned g2t[8] = {0x76543120u, 0x76542301u, 0x76534210u, 0x76453201u, 0x75643210u, 0x67543201u, 0x76543210u, 0x76543201u};
    const unsigned h01 = (bi & 4) ? g2t[1] : g2t[0], h23 = (bi & 4) ? g2t[3] : g2t[2], h45 = (bi & 4) ? g2t[5] : g2t[4], h67 = (bi & 4) ? g2t[7] : g2t[6];
    return (bi & 16) ? ((bi & 8) ? h67 : h45) : ((bi & 8) ? h23 : h01);
  }
  DI int head_at(unsigned ho, int j) const { return (int)(2 * (ho >> (4 * (j >> 1))) + (j & 1) + 4 * (bi & 3)) & 15; }
  DI unsigned vwo() const { return (lane >> 3) * 2048 + (((lane & 7) ^ ((blk << 2) | (h << 1))) << 4); }
  DI unsigned ta(unsigned lds, int u) const {
    const int s = 8 * h + 4 * u + q, sg = (((s >> 1) & 1) << 2) | (s & 1) | (((s >> 2) & 1) << 1);
    return lds + PK_OFF + wr * 16384 + s * 128 + (((hp * 4 + blk * 2 + (p & 1)) ^ sg) << 4) + (p >> 1) * 8;
  }
};
#define IDS(wave) const Ids I(wave); [[maybe_unused]] const int b = I.b, bi = I.bi, rt = I.rt, ct = I.ct, rt2 = I.rt2, ct2 = I.ct2, cw = I.cw, wr = I.wr, hp = I.hp, \
  lane = I.lane, r = I.r, h = I.h, q = I.q, p = I.p, blk = I.blk, G = I.G, l15 = I.l15
#define WO_PIECE(pos) glds(ro, lds + WO_OFF + (pos) * 4096 + cw * 1024, vwo, (((pos) & 16) | I.head_at(ho, ((pos) - PROT) & 15)) * 65536 + cw * 16384 + ct2 * 128)

__global__ __launch_bounds__(512, 2) void hedgehog_fused(const u16* __restrict__ x, const u16* __restrict__ w, const u16* __restrict__ wphi,
                                                         const u16* __restrict__ wo, u16* __restrict__ out, char* __restrict__ ws) {
  extern __shared__ __attribute__((aligned(16))) char smem[];
  const unsigned lds = (unsigned)(size_t)(LDS3 char*)smem;
  const int wave = __builtin_amdgcn_readfirstlane(threadIdx.x >> 6);
  asm volatile("s_cmp_gt_u32 %1, 3\n\ts_cbranch_scc1 1f\n\ts_load_dwordx2 s[88:89], %0, 0\n\ts_load_dwordx2 s[92:93], %0, 8\n\ts_load_dwordx2 s[96:97], %0, 40\n1:"
               ::"s"(__builtin_amdgcn_kernarg_segment_ptr()), "s"(wave) : "scc", "s88", "s89", "s92", "s93", "s96", "s97");
  {
    unsigned len, t; u64 pc;
    asm volatile("s_getpc_b64 %0\n1:\n\ts_mov_b32 %1, .Lhedgehog_tail0-1b\n\ts_max_u32 %1, %1, .Lhedgehog_tail1-1b\n\ts_max_u32 %1, %1, .Lhedgehog_tail2-1b" : "=s"(pc), "=s"(len)::"scc");
    const rsrc_t rc = __builtin_amdgcn_make_buffer_rsrc((void*)pc, 0, len, 0x00020000);
    asm volatile("s_cmp_lg_u32 %4, 0\n\ts_cbranch_scc1 1f\n\ts_mov_b32 m0, %1\n\tv_lshl_add_u32 %0, %2, 12, %5\n\tbuffer_load_dword %0, %3, 0 offen lds\n1:"
                 : "=&v"(t) : "s"(lds + 7168), "v"(threadIdx.x & 63), "s"(rc), "s"(wave), "s"((blockIdx.x >> 3) * 128) : "memory", "scc"); }
  const Ids I0(wave);
  const unsigned va = (I0.lane >> 3) * 2048 + (((I0.lane & 7) ^ ((4 * I0.hp + I0.G) & 7)) << 4);
  const unsigned vb = (I0.lane >> 3) * 6144 + (((I0.lane & 7) ^ ((I0.blk << 2) | (I0.hp << 1))) << 4);
  const bool hs = I0.lane < 16 && I0.cw < 3;
  const unsigned a_msg_in = hs ? WS_MSG + I0.b * 512 + I0.cw * 128 + I0.lane * 8 : OOB;
  const unsigned sa0 = (I0.rt * 128 + I0.cw * 8) * 2048, sb0 = I0.cw * 8 * 6144 + I0.ct * 128;
  const int g0 = (I0.rt >> 1) & 2;
  u64 px, pw, pws;
  u64x2 pow;
  { constexpr unsigned first = (ORD0 & 15) | (ORD1 & 15) << 4 | (ORD2 & 15) << 8 | (ORD3 & 15) << 12;
    const unsigned kt = (first >> (((I0.ct - I0.rt) & 3) * 4)) & 15, dst = lds + ST_OFF + I0.cw * 1024, sb = sb0 + kt * (64 * 6144);
    unsigned t;
#define E_LD(d, dl, v, r, s, sl) "s_add_u32 m0, %[" #d "], " #dl "\n\ts_add_u32 %[t], %[" #s "], " #sl "\n\tbuffer_load_dwordx4 %[" #v "], " r ", %[t] offen lds\n\t"
    asm volatile("s_cmp_gt_u32 %[wave], 3\n\ts_cbranch_scc1 1f\n\t"
                 "s_mov_b32 s90, 0x400000\n\ts_mov_b32 s91, 0x20000\n\ts_mov_b32 s94, 0x600000\n\ts_mov_b32 s95, 0x20000\n\ts_mov_b32 s98, %[end]\n\ts_mov_b32 s99, 0x20000\n\t"
                 "s_waitcnt lgkmcnt(0)\n\ts_and_b32 s97, s97, 0xffff\n\ts_and_b32 s89, s89, 0xffff\n\tbuffer_load_dwordx2 v[254:255], %[msg], s[96:99], 0 offen sc1\n\ts_and_b32 s93, s93, 0xffff\n\t"
                 E_LD(d, 0, va, "s[88:91]", sa, 0) E_LD(d, 4096, va, "s[88:91]", sa, 65536) E_LD(d, 8192, va, "s[88:91]", sa, 131072) E_LD(d, 12288, va, "s[88:91]", sa, 196608)
                 E_LD(d0, 0, vb, "s[92:95]", s0, 0) E_LD(d0, 4096, vb, "s[92:95]", s0, 196608) E_LD(d, 24576, vb, "s[92:95]", s1, 0) E_LD(d, 28672, vb, "s[92:95]", s1, 196608)
                 E_LD(d2, 0, vb, "s[92:95]", s2, 0) E_LD(d2, 4096, vb, "s[92:95]", s2, 196608) "1:"
                 : [t] "=&s"(t), "=&{s[88:89]}"(px), "=&{s[92:93]}"(pw), "=&{s[96:97]}"(pws)
                 : [wave] "s"(wave), [msg] "v"(a_msg_in), [end] "n"(WS_END), [va] "v"(va), [vb] "v"(vb), [d] "s"(dst), [sa] "s"(sa0 + kt * 128),
                   [d0] "s"(dst + BOFF + g0 * 8192), [s0] "s"(sb + g0 * 2048), [s1] "s"(sb + 2048), [d2] "s"(dst + BOFF + 16384 - g0 * 8192), [s2] "s"(sb + 4096 - g0 * 2048)
                 : "memory", "scc", "v254", "v255", "s90", "s91", "s94", "s95", "s98", "s99"); }
  unsigned wb[4], xw, xr, vo, l16g, vfg, so2, sk, ss, sfg, ho2;
  bf16x8 wv[16][4];
  auto g2_setup = [&] {
    IDS(wave);
    const int kh = wave >> 2;
#pragma unroll
    for (int n = 0; n < 4; n++) wb[n] = lds + WO_OFF + kh * 65536 + (16 * (G >> 1) + 4 * (G & 1) + q) * 128 + (((n * 2 + (p >> 1)) ^ ((((q >> 1) & 1) << 2) | ((G & 1) << 1))) << 4) + (p & 1) * 8;
    xw = lds + wave * 4096 + lane * 16; xr = lds + (wave ^ 4) * 4096 + lane * 16; vo = l15 * 2048 + (G & 1) * 32 + (G >> 1) * 16; l16g = lane * 16;
    so2 = (rt2 * 128 + cw * 32 + kh * 16) * 2048 + ct2 * 128;
    sk = WS_O + ((rt2 * 8 + cw * 2 + kh) * 32 + kh * 16) * 1024; ss = WS_O + ((rt2 * 8 + cw * 2 + 1 - kh) * 32 + kh * 16) * 1024;
    vfg = (l15 >> 3) * 2048 + (l15 & 7) * 128; sfg = WS_OFLAG + ((rt2 * 4 + (cw & 2)) * 16 + kh * 8) * 128;
    ho2 = (I.hord() + (bi & 3) * 0x22222222u) & 0x77777777u;
    asm volatile("" : "+v"(wb[0]), "+v"(wb[1]), "+v"(wb[2]), "+v"(wb[3]), "+v"(xw), "+v"(xr), "+v"(vo), "+v"(l16g), "+v"(vfg));
    asm volatile("" : "+s"(ho2), "+s"(so2), "+s"(sk), "+s"(ss), "+s"(sfg));
    SCHED0();
#pragma unroll
    for (int j = 0; j < BAH; j++) {
#pragma unroll
      for (int n = 0; n < 4; n++) wv[j][n] = tr2(wb[n] + ((j + PROT) & 15) * 4096, wb[n] + ((j + PROT) & 15) * 4096 + 1024);
      SCHED0();
    }
  };
  if (__builtin_expect(wave < 4, 1)) {
    IDS(wave);
    const rsrc_t rs = __builtin_amdgcn_make_buffer_rsrc((void*)pws, 0, WS_END, 0x00020000);
    const rsrc_t rx = __builtin_amdgcn_make_buffer_rsrc((void*)px, 0, 2048 * 1024 * 2, 0x00020000);
    const rsrc_t rw = __builtin_amdgcn_make_buffer_rsrc((void*)pw, 0, 1024 * 3072 * 2, 0x00020000);
    const int cls = ct - rt;
    u64 order = (cls & 2) ? ((cls & 1) ? ORD3 : ORD2) : ((cls & 1) ? ORD1 : ORD0);
    asm volatile("" : "+s"(order));
    const u64x2 pp = karg2<16>();
    const u64 wphi_ = pp.x, wo_ = pp.y;
    pow = karg2<32>();
    struct Step { unsigned dst, sa, sb; };
    auto step = [&](int t, int slot) { const int kt = (int)(order >> (4 * t)) & 15; return Step{lds + ST_OFF + slot * STAGE + cw * 1024, sa0 + kt * 128, sb0 + kt * (64 * 6144)}; };
    auto issue_a = [&](const Step& u) { glds2<4096, 65536>(rx, u.dst, va, u.sa); glds2<4096, 65536>(rx, u.dst + 8192, va, u.sa + 131072); };
    auto issue_b = [&](const Step& u) {
      glds2<4096, 196608>(rw, u.dst + BOFF + g0 * 8192, vb, u.sb + g0 * 2048);
      glds2<4096, 196608>(rw, u.dst + BOFF + 8192, vb, u.sb + 2048);
      glds2<4096, 196608>(rw, u.dst + BOFF + 16384 - g0 * 8192, vb, u.sb + 4096 - g0 * 2048);
    };
    auto issue = [&](int t, int slot) { const Step u = step(t, slot); issue_a(u); issue_b(u); };
    const rsrc_t ro = __builtin_amdgcn_make_buffer_rsrc((void*)wo_, 0, 1024 * 1024 * 2, 0x00020000);
    const rsrc_t rp = __builtin_amdgcn_make_buffer_rsrc((void*)wphi_, 0, 32 * 32 * 32 * 2, 0x00020000);
    const int peer = cw == 0 ? blk_of1(l15, ct) : cw == 1 ? blk_of1(rt2, l15) : blk_of2(rt, l15), pg = cw == 0 ? 0 : 3 - cw;
    unsigned a_msg_out = hs ? WS_MSG + peer * 512 + pg * 128 + (cw == 0 ? rt : cw == 1 ? ct2 : ct) * 8 : OOB;
    unsigned a_nonce = hs ? WS_NONCE + (pg * 256 + peer) * 8 : OOB, lane0 = lane == 0 ? 0 : OOB, ones = ~0u, cv = 0u - wr, vwp = (16 * h + 8 * blk + q) * 64 + p * 16;
    unsigned vwo = I.vwo(), l16 = lane * 16, ho = I.hord();
    unsigned vfl = lane < 32 ? WS_OFLAG + ((rt * 4 + cw) * 16 + ct) * 128 + lane * 4 : cw == 0 ? WS_HINT + (rt * 16 + ct) * 128 + r * 4 : OOB;
    asm volatile("" : "+v"(a_msg_out), "+v"(a_nonce), "+v"(lane0), "+v"(ones), "+v"(cv), "+v"(vwp), "+v"(vwo), "+v"(l16), "+v"(vfl));
    asm volatile("" : "+s"(ho));
    issue(1, 1);
    unsigned nonce = 1;
    waitv<10>(); bar();
    issue(2, 2);
    { const unsigned m = ld2_get().x;
      while (__builtin_amdgcn_ballot_w64(m == nonce)) nonce++; }
    __builtin_amdgcn_raw_buffer_store_b32(nonce, rs, lane0, WS_NONCE + (cw * 256 + b) * 8, SC1);
    int slot = 0;
    unsigned spo = (((rt2 >> 2) * 32 + bi) * 8 + cw * 2) * 2048 + (ct2 >> 3) * 1024;
    Step u = step(3, 0);
    asm volatile("s_waitcnt vmcnt(11)\n\ts_barrier" : "+s"(u.dst), "+s"(u.sa), "+s"(u.sb)::"memory");
    auto ring = [&](int t, auto with_pf) {
      issue_a(u);
      const Step v = u;
      slot = slot == NS - 1 ? 0 : slot + 1;
      u = step((t + 4) & 15, slot);
      asm volatile("" : "+s"(u.dst), "+s"(u.sa), "+s"(u.sb));
      if (decltype(with_pf)::value) {
        spf<0>(ro, spo); spf<64>(ro, spo); spf<2048>(ro, spo); spf<2112>(ro, spo);
        spo += 128;
      }
      issue_b(v);
      waitv<10>(); bar();
    };
#pragma unroll 1
    for (int t = 0; t < SPF0; t++) ring(t, Int<0>{});
    spf<0>(rp, ct * 4096 + rt * 256 + cw * 64);
#pragma unroll 1
    for (int t = SPF0; t < SPF0 + 8; t++) ring(t, Int<1>{});
#pragma unroll 1
    for (int t = SPF0 + 8; t < 13; t++) ring(t, Int<0>{});
    st16((u32x4){ones, ones, ones, ones}, rs, l16, WS_IN + b * 8192 + cw * 2048);
    st16<1024>((u32x4){ones, ones, ones, ones}, rs, l16, WS_IN + b * 8192 + cw * 2048);
    __builtin_amdgcn_raw_buffer_store_b32(cv, rs, l16 >> 2, (cw < 2 ? WS_Z + b * 512 : WS_KVR + ct * 8192 + rt * 512) + hp * 256, SC1);
    __builtin_amdgcn_raw_buffer_store_b32(0u, rs, vfl, 0, SC1);
    ld2_async(rs, a_nonce);
    glds(rp, lds + WPHI_OFF + cw * 1024, vwp, ct * 4096 + wr * 2048 + hp * 256);
#define WO_EARLY(i) WO_PIECE((i) < 8 ? 24 + (i) : (i) < 26 ? (i) - 2 : (i) - 26)
#pragma unroll
    for (int i = 0; i < WO1; i++) WO_EARLY(i);
    waitv<6 + WO1>(); bar();
    {
      const unsigned so = WS_O + ((rt * 8 + cw * 2) * 32 + ct * 2) * 1024, vo = ((ct - 2 * rt) & 7) < 4 ? l16 : OOB;
      st16((u32x4){ones, ones, ones, ones}, rs, vo, so); st16<1024>((u32x4){ones, ones, ones, ones}, rs, vo, so);
      st16((u32x4){ones, ones, ones, ones}, rs, vo, so + 32768); st16<1024>((u32x4){ones, ones, ones, ones}, rs, vo, so + 32768); }
#pragma unroll
    for (int i = WO1; i < WO2; i++) WO_EARLY(i);
    waitv<WO2 + 4>(); bar();
    unsigned echoed = ld2_get().x;
    if (cw == 0) __builtin_amdgcn_raw_buffer_store_b64((u32x2){echoed, nonce}, rs, a_msg_out, 0, SC1);
    __builtin_amdgcn_s_sleep(WOSLP);
#pragma unroll
    for (int i = WO2; i < WO3; i++) WO_EARLY(i);
    auto shake = [&]() {
      for (unsigned spins = 0;; spins++) {
        const u32x2 m = __builtin_amdgcn_raw_buffer_load_b64(rs, a_msg_in, 0, SC1);
        const bool ok = !hs || m.x == nonce;
        if (ok && hs && m.y != echoed) { echoed = m.y; __builtin_amdgcn_raw_buffer_store_b64((u32x2){echoed, nonce}, rs, a_msg_out, 0, SC1); }
        if (__builtin_expect(__builtin_amdgcn_ballot_w64(!ok) == 0, 1) || spins > SPIN_CAP) break;
        asm volatile("s_sleep 4" ::: "memory");
      }
    };
    if (cw == 0) { waitv<0>(); ld2_async(rs, a_msg_in); }
    const unsigned ta0 = I.ta(lds, 0), ta1 = I.ta(lds, 1);
    bar();
    waitv<0>();
    if (cw == 0) {
      const u32x2 m = ld2_get();
      if (__builtin_expect(__builtin_amdgcn_ballot_w64(hs && (m.x != nonce || m.y != echoed)) != 0, 0)) shake();
      if (lane == 0) lds_st(lds + ZFLAG_OFF + 1024, 1u);
    }
    {
      bf16x4 uu[16];
#pragma unroll
      for (int i = 0; i < 16; i++) uu[i] = trd(((i & 1) ? ta1 : ta0) + (i >> 1) * 2048);
      SCHED0();
      float z[4] = {0.f, 0.f, 0.f, 0.f};
      const bf16x2 one = {(__bf16)1.0f, (__bf16)1.0f};
#pragma unroll
      for (int i = 0; i < 16; i++) {
        z[2 * (i & 1)] = __builtin_amdgcn_fdot2_f32_bf16(__builtin_shufflevector(uu[i], uu[i], 0, 1), one, z[2 * (i & 1)], false);
        z[2 * (i & 1) + 1] = __builtin_amdgcn_fdot2_f32_bf16(__builtin_shufflevector(uu[i], uu[i], 2, 3), one, z[2 * (i & 1) + 1], false);
      }
      __builtin_amdgcn_raw_buffer_store_b32(__float_as_uint(xhalf_add((z[0] + z[1]) + (z[2] + z[3]))) | 1u, rs, lane < 32 ? lane * 4 : OOB, WS_Z + b * 512 + cw * 128, SC1);
    }
    bar();
    if (cw != 0) __builtin_amdgcn_raw_buffer_store_b64((u32x2){echoed, nonce}, rs, a_msg_out, 0, SC1);
    __builtin_amdgcn_s_setprio(2);
#pragma unroll
    for (int i = WO3; i < WO4; i++) WO_EARLY(i);
    unsigned vin = h * 4096 + r * 4, vst = lane < 32 ? lane * 4 : OOB, vz = h * 512 + r * 4, kfw = lds + KVF_OFF + cw * 2048 + lane * 16;
    unsigned zw = lds + ZF_OFF + cw * 64 + r * 2, zfw = lds + ZFLAG_OFF + cw * 256 + lane * 4;
    unsigned s_in = WS_IN + b * 8192 + cw * 128, s_red = WS_KVR + ct * 8192, s_z = WS_Z + blk_of1(0, ct) * 512 + cw * 128;
    asm volatile("" : "+v"(vin), "+v"(vst), "+v"(vz), "+v"(kfw), "+v"(zw), "+v"(zfw));
    asm volatile("" : "+s"(s_in), "+s"(s_red), "+s"(s_z));
    __builtin_amdgcn_s_sleep(PACE1);
    unsigned y[8];
    {
      unsigned v0, v1, v2, v3, v4, v5, v6, v7, mx, s0, s1, cnt, sz;
#define H1_LD(j) "buffer_load_dword %[v" #j "], %[vin], %[rs], %[sin] offen offset:" #j "*512 sc1\n\t"
#define H1_LZ(j) "buffer_load_dword %[y" #j "], %[vz], %[rs], %[sz] offen sc1\n\ts_add_u32 %[sz], %[sz], 4096\n\t"
#define H1_AC(j) "v_dot2c_f32_bf16 %[s0], %[k0], %[v" #j "]\n\tv_dot2c_f32_bf16 %[s1], %[k1], %[v" #j "]\n\t"
      asm volatile("s_mov_b32 %[cnt], 0\n1:\n\t" H1_LD(0) H1_LD(1) H1_LD(2) H1_LD(3) H1_LD(4) H1_LD(5) H1_LD(6) H1_LD(7)
                   "s_mov_b32 %[sz], %[sz0]\n\t" H1_LZ(0) H1_LZ(1) H1_LZ(2) H1_LZ(3) H1_LZ(4) H1_LZ(5) H1_LZ(6) H1_LZ(7)
                   "v_mov_b32 %[s0], 0\n\tv_mov_b32 %[s1], 0\n\ts_add_u32 %[cnt], %[cnt], 1\n\ts_cmp_gt_u32 %[cnt], 0x40000\n\t"
                   "s_waitcnt vmcnt(14)\n\t" H1_AC(0) H1_AC(1) "v_max_u32 %[mx], %[v0], %[v1]\n\t"
                   "s_waitcnt vmcnt(12)\n\t" H1_AC(2) H1_AC(3) "v_max3_u32 %[mx], %[mx], %[v2], %[v3]\n\t"
                   "s_waitcnt vmcnt(10)\n\t" H1_AC(4) H1_AC(5) "v_max3_u32 %[mx], %[mx], %[v4], %[v5]\n\t"
                   "s_waitcnt vmcnt(8)\n\t" H1_AC(6) H1_AC(7) "v_max3_u32 %[mx], %[mx], %[v6], %[v7]\n\t"
                   "v_cmp_eq_u32 vcc, -1, %[mx]\n\ts_cbranch_scc1 2f\n\ts_cbranch_vccnz 1b\n2:\n\t"
                   "v_mov_b32 %[v0], %[s0]\n\tv_mov_b32 %[v1], %[s1]\n\ts_nop 0\n\tv_permlane32_swap_b32 %[s0], %[v0]\n\tv_permlane32_swap_b32 %[s1], %[v1]\n\t"
                   "v_add_f32 %[s0], %[s0], %[v0]\n\tv_add_f32 %[s1], %[s1], %[v1]\n\tv_cvt_pk_bf16_f32 %[s0], %[s0], %[s1]\n\tv_min_u32 %[s0], -2, %[s0]\n\t"
                   "buffer_store_dword %[s0], %[vst], %[rs], %[sst] offen sc1\n\ts_waitcnt vmcnt(1)"
                   : [v0] "=&v"(v0), [v1] "=&v"(v1), [v2] "=&v"(v2), [v3] "=&v"(v3), [v4] "=&v"(v4), [v5] "=&v"(v5), [v6] "=&v"(v6), [v7] "=&v"(v7),
                     [mx] "=&v"(mx), [s0] "=&v"(s0), [s1] "=&v"(s1), [cnt] "=&s"(cnt), [sz] "=&s"(sz),
                     [y0] "=&v"(y[0]), [y1] "=&v"(y[1]), [y2] "=&v"(y[2]), [y3] "=&v"(y[3]), [y4] "=&v"(y[4]), [y5] "=&v"(y[5]), [y6] "=&v"(y[6]), [y7] "=&v"(y[7])
                   : [vz] "v"(vz), [sz0] "s"(s_z), [vin] "v"(vin), [vst] "v"(vst), [rs] "s"(rs), [sin] "s"(s_in), [sst] "s"(s_red + rt * 512 + cw * 128), [k0] "s"(0x00003f80u), [k1] "s"(0x3f800000u)
                   : "memory", "vcc", "scc");
    }
    { float z;
      for (unsigned spins = 0;; spins++) {
        const unsigned ok = y[0] & y[1] & y[2] & y[3] & y[4] & y[5] & y[6] & y[7] & 1u;
        const f32x2 t = ((f32x2){__uint_as_float(y[0]), __uint_as_float(y[1])} + (f32x2){__uint_as_float(y[2]), __uint_as_float(y[3])}) +
                        ((f32x2){__uint_as_float(y[4]), __uint_as_float(y[5])} + (f32x2){__uint_as_float(y[6]), __uint_as_float(y[7])});
        z = t.x + t.y;
        if (__builtin_expect(__builtin_amdgcn_ballot_w64(ok == 0) == 0, 1) || spins > SPIN_CAP) break;
        asm volatile("" ::: "memory");
#pragma unroll
        for (int j = 0; j < 8; j++) y[j] = __builtin_amdgcn_raw_buffer_load_b32(rs, vz, s_z + j * 4096, SC1);
      }
      z = xhalf_add(z);
      asm volatile("ds_write_b16 %0, %1\n\tds_write_b32 %2, %2" ::"v"(zw), "v"(pk2(z, z)), "v"(zfw) : "memory");
    }
    __builtin_amdgcn_s_sleep(PACE2);
    {
      unsigned mx, cnt;
      asm volatile("s_mov_b32 %[cnt], 0\n1:\n\tbuffer_load_dwordx4 v[48:51], %[l16], %[rs], %[so] offen sc1\n\tbuffer_load_dwordx4 v[52:55], %[l16], %[rs], %[so] offen offset:1024 sc1\n\t"
                   "s_add_u32 %[cnt], %[cnt], 1\n\ts_cmp_gt_u32 %[cnt], %[cap]\n\t"
                   "s_waitcnt vmcnt(1)\n\tds_write_b128 %[kfw], v[48:51]\n\tv_max_u32 %[mx], v48, v49\n\tv_max3_u32 %[mx], %[mx], v50, v51\n\t"
                   "s_waitcnt vmcnt(0)\n\tds_write_b128 %[kfw], v[52:55] offset:1024\n\tv_max3_u32 %[mx], %[mx], v52, v53\n\tv_max3_u32 %[mx], %[mx], v54, v55\n\t"
                   "v_cmp_eq_u32 vcc, -1, %[mx]\n\ts_waitcnt lgkmcnt(0)\n\ts_cbranch_scc1 2f\n\ts_cbranch_vccnz 1b\n2:"
                   : [mx] "=&v"(mx), [cnt] "=&s"(cnt)
                   : [l16] "v"(l16), [kfw] "v"(kfw), [rs] "s"(rs), [so] "s"(s_red + cw * 2048), [cap] "n"(SPIN_CAP)
                   : "memory", "vcc", "scc", "v48", "v49", "v50", "v51", "v52", "v53", "v54", "v55");
    }
    lgkm0_bar();
    __builtin_amdgcn_s_setprio(0);
    if (cw == 0) __builtin_amdgcn_raw_buffer_store_b32(1u, rs, lane0, WS_HINT + (rt * 16 + ct) * 128, SC1);
    ld2_async(rs, a_msg_in);
    g2_setup();
    waitv<0>();
    { const u32x2 m = ld2_get();
      if (__builtin_expect(__builtin_amdgcn_ballot_w64(hs && (m.x != nonce || m.y != echoed)) != 0, 0)) shake(); }
#pragma unroll
    for (int i = WO4; i < 32; i++) WO_EARLY(i);
    if (cw == 1) poll1(rs, l15 * 128, WS_HINT + rt2 * 2048);
    asm volatile(".Lhedgehog_tail0:\n\ts_waitcnt vmcnt(0)" ::: "memory");
  } else {
    IDS(wave);
    unsigned aa[2], bb[2];
#pragma unroll
    for (int s = 0; s < 2; s++) {
      aa[s] = lds + ST_OFF + (wr * 64 + l15) * 128 + (((4 * s + G) ^ (l15 >> 1)) << 4);
      bb[s] = lds + ST_OFF + BOFF + (8 * G + q) * 128 + (((hp * 4 + s * 2 + (p >> 1)) ^ ((((q >> 1) & 1) << 2) | ((G & 1) << 1))) << 4) + (p & 1) * 8;
    }
    if (lane < 2) lds_st(lds + ZFLAG_OFF + (lane ? 1024 : cw * 256), 0u);
    const unsigned wfa = lds + WPHI_OFF + hp * 2048 + (4 * h + q) * 64 + blk * 32 + p * 8;
    const unsigned pos = ((G & 1) << 10) + ((G >> 1) * 32 + l15) * 16;
    unsigned vf = lds + VF_OFF + (hp * 4 + wr * 2) * 2048 + pos;
    unsigned vr = lds + VF_OFF + wr * 8192 + lane * 16, kvo = blk_of1(cw * 4 + h, ct) * 8192 + r * 16, ta0 = I.ta(lds, 0), ta1 = I.ta(lds, 1), fl0 = lds + ZFLAG_OFF + 1024;
    unsigned pk0 = lds + PK_OFF + hp * 16384 + (wr * 64 + r) * 128 + ((((((r >> 1) & 1) << 2) | (r & 1) | (((r >> 2) & 1) << 1)) ^ h) << 4), s_kv = WS_IN + rt * 512;
    asm volatile("" : "+v"(vf), "+v"(vr), "+v"(kvo), "+v"(ta0), "+v"(ta1), "+v"(pk0), "+v"(fl0));
    asm volatile("" : "+s"(s_kv));
    const bool slow = (((int)karg2<0>().y >> 7) + ct & 7) == 3;
    pow = karg2<32>();
    const rsrc_t rs = __builtin_amdgcn_make_buffer_rsrc((void*)pow.y, 0, WS_END, 0x00020000);
    __builtin_amdgcn_s_setprio(1);
    f32x16 oq0, oq1, o0, o1, o2, o3;
    bf16x8 wf[2];
    { unsigned cnt, sl, dl; u64 last, tmp, ramp = slow ? unary(KRAMPS) : unary(KRAMP);
      asm volatile(G1_LOOP
                   : "=&{v[128:143]}"(oq0), "=&{v[144:159]}"(oq1), "=&{v[160:175]}"(o0), "=&{v[176:191]}"(o1), "=&{v[192:207]}"(o2), "=&{v[208:223]}"(o3), "=&{v[64:67]}"(wf[0]), "=&{v[68:71]}"(wf[1]),
                     [a0] "+v"(aa[0]), [a1] "+v"(aa[1]), [b0] "+v"(bb[0]), [b1] "+v"(bb[1]), [cnt] "=&s"(cnt), [sl] "=&s"(sl), [dl] "=&s"(dl), [last] "=&s"(last), [tmp] "=&s"(tmp), [ramp] "+s"(ramp)
                   : [w0] "v"(wfa), [w1] "v"(wfa + 1024), [pos] "s"(STAGE), [neg] "s"(-(NS - 1) * STAGE)
                   : "memory", "scc", G1_CLOB); }
    u32x4 q0, q1, q2, q3;
    asm volatile(KMAP_ASM
                 : "=&{v[48:51]}"(q0), "=&{v[52:55]}"(q1), "=&{v[56:59]}"(q2), "=&{v[60:63]}"(q3), "+{v[160:175]}"(o0), "+{v[176:191]}"(o1), "+{v[192:207]}"(o2), "+{v[208:223]}"(o3)
                 : "{v[128:143]}"(oq0), "{v[144:159]}"(oq1), "{v[64:67]}"(wf[0]), "{v[68:71]}"(wf[1]), [vf] "v"(vf), [pk] "v"(pk0), [ta0] "v"(ta0), [ta1] "v"(ta1), [vr] "v"(vr),
                   [kvo] "v"(kvo), [fl] "v"(fl0), [rs] "s"(rs), [s0] "s"(s_kv), [s1] "s"(s_kv + 16 * 8192), [cc] "s"(0x3fb8aa3bull), [nc] "s"(0xbfb8aa3bu)
                 : "memory", "vcc", KMAP_CLOB);
    u32x4 P[2][4];
    { const f32x16 pq[2] = {project(wf, q0, q1), project(wf, q2, q3)};
      dual_softmax2(pq, [&](int k, int j, u32x2 v) { P[k][j >> 1][2 * (j & 1)] = v.x; P[k][j >> 1][2 * (j & 1) + 1] = v.y; }); }
    asm volatile("" : "+v"(P[0][0]), "+v"(P[0][1]), "+v"(P[0][2]), "+v"(P[0][3]), "+v"(P[1][0]), "+v"(P[1][1]), "+v"(P[1][2]), "+v"(P[1][3]));
    unsigned vo2 = blk * 32768 + (h * 16 + l15) * 16, kfa = lds + KVF_OFF + hp * 4096 + lane * 16, lane0 = lane == 0 ? 0 : OOB;
    unsigned so = WS_O + ((rt * 8 + wr * 4) * 32 + ct * 2 + hp) * 1024;
    asm volatile("" : "+v"(vo2), "+v"(kfa), "+v"(lane0));
    asm volatile("" : "+s"(so));
    float rd[2];
    { u32x4 zf[4]; unsigned fl;
      asm volatile("1:\n\tds_read_b32 %4, %5\n\tds_read2_b64 %0, %6 offset0:0 offset1:2\n\tds_read2_b64 %1, %6 offset0:4 offset1:6\n\t"
                   "ds_read2_b64 %2, %6 offset0:8 offset1:10\n\tds_read2_b64 %3, %6 offset0:12 offset1:14\n\t"
                   "s_waitcnt lgkmcnt(0)\n\tv_cmp_eq_u32 vcc, 0, %4\n\ts_cbranch_vccnz 1b"
                   : "=&v"(zf[0]), "=&v"(zf[1]), "=&v"(zf[2]), "=&v"(zf[3]), "=&v"(fl)
                   : "v"(lds + ZFLAG_OFF + hp * 512 + h * 256), "v"(lds + ZF_OFF + hp * 128 + h * 8) : "memory", "vcc");
      float d[4] = {0.f, 0.f, 0.f, 0.f};
#pragma unroll
      for (int i = 0; i < 32; i++) {
        const int k = i & 1, e = i >> 1;
        const unsigned pa = P[k][e >> 2][e & 3], za = zf[e >> 2][e & 3];
        d[i & 3] = __builtin_amdgcn_fdot2_f32_bf16(__builtin_bit_cast(bf16x2, pa), __builtin_bit_cast(bf16x2, za), d[i & 3], false);
      }
      auto t = __builtin_amdgcn_permlane32_swap(__float_as_uint(d[0] + d[2]), __float_as_uint(d[1] + d[3]), false, false);
      const float inv = __builtin_amdgcn_rcpf(__uint_as_float(t[0]) + __uint_as_float(t[1]) + 1e-6f);
      auto ti = __builtin_amdgcn_permlane32_swap(__float_as_uint(inv), __float_as_uint(inv), false, false);
      rd[0] = __uint_as_float(ti[0]); rd[1] = __uint_as_float(ti[1]);
      asm volatile("" : "+v"(rd[0]), "+v"(rd[1])); }
    bar();
    { bf16x8 kf[4];
#pragma unroll
      for (int j = 0; j < 4; j++) kf[j] = ld128(kfa + j * 1024);
      SCHED0();
      f32x16 num[2] = {};
#pragma unroll
      for (int i = 0; i < 8; i++) num[i >> 2] = MFMA32(kf[i & 3], as8(P[i >> 2][i & 3]), num[i >> 2]);
      SCHED0();
#pragma unroll
      for (int k = 0; k < 2; k++) {
        num[k] *= rd[k];
        st16(pack8<0>(num[k]), rs, vo2, so + k * 65536);
        st16<512>(pack8<1>(num[k]), rs, vo2, so + k * 65536);
      }
      g2_setup();
      waitv<0>();
      __builtin_amdgcn_raw_buffer_store_b32(1u, rs, lane0, WS_OFLAG + ((rt * 4 + cw) * 16 + ct) * 128, SC1);
    }
    __builtin_amdgcn_s_setprio(0);
    asm volatile(".Lhedgehog_tail1:" ::: "memory");
  }
  out = (u16*)pow.x; ws = (char*)pow.y;
  const rsrc_t rout = __builtin_amdgcn_make_buffer_rsrc(out, 0, 2048 * 1024 * 2, 0x00020000);
  const rsrc_t rs = __builtin_amdgcn_make_buffer_rsrc(ws, 0, WS_END, 0x00020000);
  bar();
  u32x4 a[16][2];
  auto lda = [&](int j) {
    const unsigned hb = ((ho2 >> (4 * (j >> 1))) & 7) * 2048;
    a[j][0] = __builtin_amdgcn_raw_buffer_load_b128(rs, l16g + (j & 1) * 1024, ss + hb, SC1);
    a[j][1] = __builtin_amdgcn_raw_buffer_load_b128(rs, l16g + (j & 1) * 1024, sk + hb, SC1);
  };
#pragma unroll
  for (int j = 0; j < AHEAD; j++) lda(j);
  unsigned fg = 0;
  f32x4 c[2][4] = {};
#pragma unroll
  for (int j = 0; j < 15; j++) {
    SCHED0();
    if (j == FGJ) fg = __builtin_amdgcn_raw_buffer_load_b32(rs, vfg, sfg, SC1);
    if (j + AHEAD == NSPEC) {
      if (__builtin_expect(__builtin_amdgcn_ballot_w64(fg != 1u) != 0, 0)) poll1(rs, vfg, sfg);
      asm volatile("" ::: "memory");
    }
    if (j + AHEAD < 16) lda(j + AHEAD);
    if (j < NSPEC) {
      if (__builtin_expect(marked(a[j][0], a[j][1]), 0)) {
        const unsigned hb = ((ho2 >> (4 * (j >> 1))) & 7) * 2048 + (j & 1) * 1024;
        reload(a[j][0], a[j][1], rs, l16g, ss + hb, sk + hb);
        if (marked(a[j][0], a[j][1])) { poll1(rs, vfg, sfg); reload(a[j][0], a[j][1], rs, l16g, ss + hb, sk + hb); }
      }
    }
    SCHED0();
    if (j + BAH < 16) {
#pragma unroll
      for (int n = 0; n < 4; n++) wv[j + BAH][n] = tr2(wb[n] + ((j + BAH + PROT) & 15) * 4096, wb[n] + ((j + BAH + PROT) & 15) * 4096 + 1024);
    }
    SCHED0();
#pragma unroll
    for (int n = 0; n < 4; n++) {
      c[0][n] = MFMA16(wv[j][n], as8(a[j][0]), c[0][n]);
      c[1][n] = MFMA16(wv[j][n], as8(a[j][1]), c[1][n]);
    }
  }
  SCHED0();
#pragma unroll
  for (int n = 0; n < 4; n++) c[0][n] = MFMA16(wv[15][n], as8(a[15][0]), c[0][n]);
#pragma unroll
  for (int n = 0; n < 4; n++) c[1][n] = MFMA16(wv[15][n], as8(a[15][1]), c[1][n]);
  SCHED0();
#pragma unroll
  for (int n = 0; n < 4; n++) *(LDS3 f32x4*)(size_t)(xw + n * 1024) = c[0][n];
  lgkm0_bar();
  { f32x4 o4[4];
#pragma unroll
    for (int n = 0; n < 4; n++) o4[n] = *(LDS3 const f32x4*)(size_t)(xr + n * 1024);
    SCHED0();
#pragma unroll
    for (int n = 0; n < 4; n += 2) {
      const f32x4 u = o4[n] + c[1][n], v = o4[n + 1] + c[1][n + 1];
      auto s0 = __builtin_amdgcn_permlane16_swap(pk2(u[0], u[1]), pk2(v[0], v[1]), false, false);
      auto s1 = __builtin_amdgcn_permlane16_swap(pk2(u[2], u[3]), pk2(v[2], v[3]), false, false);
      if (n) st16<64>((u32x4){s0[0], s1[0], s0[1], s1[1]}, rout, vo, so2); else st16((u32x4){s0[0], s1[0], s0[1], s1[1]}, rout, vo, so2);
    }
  }
  asm volatile(".Lhedgehog_tail2:" ::: "memory");
}

extern "C" void kernel_launch(void* const* d_in, const int* in_sizes, int n_in, void* d_out, int out_size, void* d_ws, size_t ws_size, hipStream_t stream) {
  (void)hipFuncSetAttribute((const void*)hedgehog_fused, hipFuncAttributeMaxDynamicSharedMemorySize, LDS_BYTES);
  hedgehog_fused<<<256, 512, LDS_BYTES, stream>>>((const u16*)d_in[0], (const u16*)d_in[1], (const u16*)d_in[2], (const u16*)d_in[3], (u16*)d_out, (char*)d_ws);
}
```
